```python
import torch

def _softmax(x, axis=-1):
    return torch.softmax(x, dim=axis)
def _gelu(x):
    return torch.nn.functional.gelu(x, approximate="tanh")
def _silu(x):
    return torch.nn.functional.silu(x)
def _sigmoid(x):
    return torch.sigmoid(x)
def _layernorm(x, axis=-1, eps=1e-5):
    m = x.mean(dim=axis, keepdim=True)
    v = x.var(dim=axis, keepdim=True, unbiased=False)
    return (x - m) / torch.sqrt(v + eps)
def _scatter_add(tgt, idx, src, axis=0):
    return tgt.clone().index_add_(axis, idx, src)
def _segment_sum(src, seg, n):
    out = torch.zeros((n,) + src.shape[1:], dtype=src.dtype)
    return out.index_add_(0, seg, src)
def _pad(x, pw):
    flat = tuple(v for pair in reversed(pw) for v in pair)
    return torch.nn.functional.pad(x, flat)

kernel_name = 'concat_heads__gqa_block_cached__gqa_block_cached__near_ridge'

def setup_inputs():

    _w = (lambda a: (lambda t: t.to(torch.bfloat16) if t.is_floating_point() else t)(torch.as_tensor(a)))
    def _A_setup():
        B, Sq, D = 8, 32, 512
        Hq, Hkv, Sk, Dh = 32, 8, 2048, 128
        assert Hq % Hkv == 0, (Hq, Hkv)
        return {
            "x": _w((lambda *s: torch.randn(*s, dtype=torch.bfloat16) * 0.8)(B, Sq, D)),
            "k_cache": _w((lambda *s: torch.randn(*s, dtype=torch.bfloat16) * 0.8)(B, Hkv, Sk, Dh)),
            "v_cache": _w((lambda *s: torch.randn(*s, dtype=torch.bfloat16) * 0.05)(B, Hkv, Sk, Dh)),
            "wq": _w((lambda *s: torch.randn(*s, dtype=torch.bfloat16) * 0.8)(D, Hq * Dh) * (D ** -0.5)),
            "wo": _w((lambda *s: torch.randn(*s, dtype=torch.bfloat16) * 0.05)(Hq * Dh, D) * ((Hq * Dh) ** -0.5)),
        }
    _A_in = _A_setup()

    def _B_setup():
        B, Sq, D = 8, 32, 512
        Hq, Hkv, Sk, Dh = 32, 8, 2048, 128
        assert Hq % Hkv == 0, (Hq, Hkv)
        return {
            "x": _w((lambda *s: torch.randn(*s, dtype=torch.bfloat16) * 0.8)(B, Sq, D)),
            "k_cache": _w((lambda *s: torch.randn(*s, dtype=torch.bfloat16) * 0.8)(B, Hkv, Sk, Dh)),
            "v_cache": _w((lambda *s: torch.randn(*s, dtype=torch.bfloat16) * 0.05)(B, Hkv, Sk, Dh)),
            "wq": _w((lambda *s: torch.randn(*s, dtype=torch.bfloat16) * 0.8)(D, Hq * Dh) * (D ** -0.5)),
            "wo": _w((lambda *s: torch.randn(*s, dtype=torch.bfloat16) * 0.05)(Hq * Dh, D) * ((Hq * Dh) ** -0.5)),
        }
    _B_in = _B_setup()

    return {
        "A_x": _A_in["x"],
        "A_k_cache": _A_in["k_cache"],
        "A_v_cache": _A_in["v_cache"],
        "A_wq": _A_in["wq"],
        "A_wo": _A_in["wo"],
        "B_x": _B_in["x"],
        "B_k_cache": _B_in["k_cache"],
        "B_v_cache": _B_in["v_cache"],
        "B_wq": _B_in["wq"],
        "B_wo": _B_in["wo"],
    }

def reference(A_x, A_k_cache, A_v_cache, A_wq, A_wo, B_x, B_k_cache, B_v_cache, B_wq, B_wo):
    def _A_ref(x, k_cache, v_cache, wq, wo):
        B, Sq, D = x.shape
        Hq, Dh = 32, 128
        Hkv = k_cache.shape[1]
        rep = Hq // Hkv
        q = (lambda x, *d: x.permute(*d))((x @ wq).reshape(B, Sq, Hq, Dh), 0, 2, 1, 3)
        k = (lambda x, n, axis: torch.repeat_interleave(x, n, dim=axis))(k_cache, rep, axis=1)
        v = (lambda x, n, axis: torch.repeat_interleave(x, n, dim=axis))(v_cache, rep, axis=1)
        scores = torch.einsum("bhqd,bhkd->bhqk", q, k) * (Dh ** -0.5)
        p = _softmax(scores, axis=-1)
        o = torch.einsum("bhqk,bhkd->bhqd", p, v)
        return (lambda x, *d: x.permute(*d))(o, 0, 2, 1, 3).reshape(B, Sq, Hq * Dh) @ wo

    def _B_ref(x, k_cache, v_cache, wq, wo):
        B, Sq, D = x.shape
        Hq, Dh = 32, 128
        Hkv = k_cache.shape[1]
        rep = Hq // Hkv
        q = (lambda x, *d: x.permute(*d))((x @ wq).reshape(B, Sq, Hq, Dh), 0, 2, 1, 3)
        k = (lambda x, n, axis: torch.repeat_interleave(x, n, dim=axis))(k_cache, rep, axis=1)
        v = (lambda x, n, axis: torch.repeat_interleave(x, n, dim=axis))(v_cache, rep, axis=1)
        scores = torch.einsum("bhqd,bhkd->bhqk", q, k) * (Dh ** -0.5)
        p = _softmax(scores, axis=-1)
        o = torch.einsum("bhqk,bhkd->bhqd", p, v)
        return (lambda x, *d: x.permute(*d))(o, 0, 2, 1, 3).reshape(B, Sq, Hq * Dh) @ wo

    a = _A_ref(A_x, A_k_cache, A_v_cache, A_wq, A_wo)
    b = _B_ref(B_x, B_k_cache, B_v_cache, B_wq, B_wo)
    return torch.concatenate([a, b], axis=-1)
```

```cpp
#include <hip/hip_runtime.h>
#include <stdint.h>

typedef __attribute__((ext_vector_type(8))) short bf16x8;
typedef __attribute__((ext_vector_type(4))) short s16x4;
typedef __attribute__((ext_vector_type(16))) float f32x16;
typedef __attribute__((ext_vector_type(4))) float f32x4;
typedef __attribute__((ext_vector_type(2))) float f32x2;
typedef __attribute__((ext_vector_type(4))) unsigned u32x4;
typedef __attribute__((ext_vector_type(2))) unsigned u32x2;
typedef __attribute__((ext_vector_type(2))) __bf16 bf16x2;
#define LAS __attribute__((address_space(3)))
#define DI __device__ __forceinline__
#define MFMA(a, b, c) __builtin_amdgcn_mfma_f32_32x32x16_bf16((a), (b), (c), 0, 0, 0)

constexpr int NB = 8, SQ = 32, DM = 512, HQ = 32, HKV = 8, SK = 2048, DH = 128;
constexpr float SCL = 0.088388347648318447f * 1.4426950408889634f;

struct Params {
  const char* x[2]; const char* k[2]; const char* v[2]; const char* wq[2]; const char* wo[2];
  char* out; char* ws;
};
constexpr size_t WS_QF = 0;
constexpr size_t WS_PO = WS_QF + (4u << 20);
constexpr size_t WS_ML = WS_PO + (16u << 20);
constexpr size_t WS_YP = WS_ML + (1u << 20);

DI unsigned rfl(unsigned v) { return __builtin_amdgcn_readfirstlane(v); }
DI unsigned cvtpk(float lo, float hi) { f32x2 v = {lo, hi}; bf16x2 b = __builtin_convertvector(v, bf16x2); return __builtin_bit_cast(unsigned, b); }
DI float swapmax(float x) { auto r = __builtin_amdgcn_permlane32_swap(__float_as_uint(x), __float_as_uint(x), false, false); return fmaxf(__uint_as_float(r[0]), __uint_as_float(r[1])); }
DI float swapadd(float x) { auto r = __builtin_amdgcn_permlane32_swap(__float_as_uint(x), __float_as_uint(x), false, false); return __uint_as_float(r[0]) + __uint_as_float(r[1]); }

typedef __amdgpu_buffer_rsrc_t rsrc_t;
DI rsrc_t mk_rsrc(const void* p, unsigned bytes) { return __builtin_amdgcn_make_buffer_rsrc((void*)p, 0, bytes, 0x00020000); }
template <bool NT> DI void dma16(rsrc_t rs, unsigned voff, unsigned soff, unsigned ldsdst) {
  if constexpr (NT) asm volatile("s_mov_b32 m0, %0\n\ts_nop 0\n\tbuffer_load_dwordx4 %1, %2, %3 offen nt lds" :: "s"(ldsdst), "v"(voff), "s"(rs), "s"(soff) : "memory");
  else              asm volatile("s_mov_b32 m0, %0\n\ts_nop 0\n\tbuffer_load_dwordx4 %1, %2, %3 offen lds" :: "s"(ldsdst), "v"(voff), "s"(rs), "s"(soff) : "memory");
}
#define WAIT_VM(n) asm volatile("s_waitcnt vmcnt(%0)" :: "i"(n) : "memory")
#define BARRIER() do { asm volatile("s_waitcnt lgkmcnt(0)\n\ts_barrier" ::: "memory"); } while (0)

DI bf16x8 lds_b128(unsigned addr) { return *(const LAS bf16x8*)(uintptr_t)addr; }
DI s16x4 lds_tr(unsigned addr) { return __builtin_amdgcn_ds_read_tr16_b64_v4i16((LAS s16x4*)(uintptr_t)addr); }
DI bf16x8 cat(s16x4 a, s16x4 b) { return __builtin_shufflevector(a, b, 0, 1, 2, 3, 4, 5, 6, 7); }
DI unsigned fsw(unsigned row) { return ((row & 3) << 2) | ((row >> 2) & 3); }

__global__ __launch_bounds__(512, 2) void k_qproj(Params p) {
  extern __shared__ __attribute__((aligned(16))) char smem[];
  const unsigned tid = threadIdx.x, lane = tid & 63, w = rfl(tid >> 6);
  const unsigned wg = blockIdx.x, kvh = wg & 7, j = wg >> 3, br = j >> 4, b = (j >> 1) & 7, hp = j & 1;
  const unsigned L0 = (unsigned)(uintptr_t)(LAS char*)smem;
  constexpr unsigned XOFF = 0, WOFF = 32768; constexpr int NST = 4;
  rsrc_t rx = mk_rsrc(p.x[br], NB * SQ * DM * 2), rw = mk_rsrc(p.wq[br], DM * HQ * DH * 2);
  const unsigned lr = lane >> 4, fl = (lr << 2) | (w & 3), chs = (lane & 15) ^ fl;
  { const unsigned vx = (4 * w + lr) * 1024 + chs * 16;
    for (int kt = 0; kt < 4; ++kt) dma16<false>(rx, vx, b * (SQ * DM * 2) + kt * 256, L0 + XOFF + kt * 8192 + w * 1024); }
  const unsigned vw = (4 * w + lr) * 8192 + chs * 16;
  const unsigned colb = (kvh * 512 + hp * 256) * 2;
  auto issue = [&](int i) {
    const unsigned kc = (i + b) & 7, slot = L0 + WOFF + (i % NST) * 32768 + w * 1024;
#pragma unroll
    for (int hh = 0; hh < 2; ++hh)
#pragma unroll
      for (int jj = 0; jj < 2; ++jj)
        dma16<false>(rw, vw, (kc * 64 + 32 * jj) * 8192 + colb + hh * 256, slot + hh * 16384 + jj * 8192);
  };
  for (int i = 0; i < NST - 1; ++i) issue(i);
  const unsigned h = lane >> 5, r = lane & 31, blk = (lane >> 4) & 1, q = (lane & 15) >> 2, pp = lane & 3;
  const unsigned hh = w >> 2, c = w & 3;
  const unsigned A0 = 256 * (8 * h + q) + 64 * (c ^ q) + 16 * ((2 * blk + (pp >> 1)) ^ (2 * h)) + 8 * (pp & 1) + hh * 16384;
  const unsigned X0 = 256 * r + 16 * (h ^ fsw(r));
  f32x16 acc = {};
  for (int i = 0; i < 8; ++i) {
    if (i + NST - 2 < 8) WAIT_VM(4 * (NST - 2)); else WAIT_VM(0);
    BARRIER();
    if (i + NST - 1 < 8) issue(i + NST - 1);
    const unsigned kc = (i + b) & 7, slot = L0 + WOFF + (i % NST) * 32768;
    const unsigned xb = L0 + XOFF + (kc >> 1) * 8192;
#pragma unroll
    for (int ks = 0; ks < 4; ++ks) {
      s16x4 a0 = lds_tr(slot + A0 + 4096 * ks), a1 = lds_tr(slot + (A0 ^ 16) + 1024 + 4096 * ks);
      bf16x8 xf = lds_b128(xb + (X0 ^ (16 * (8 * (kc & 1) + 2 * ks))));
      acc = MFMA(cat(a0, a1), xf, acc);
    }
  }
  char* qf = p.ws + WS_QF + ((size_t)((br * 8 + b) * 8 + kvh) * 4 + hp * 2 + hh) * 8192;
#pragma unroll
  for (int s = 0; s < 2; ++s) {
    unsigned a0 = cvtpk(acc[8 * s + 0], acc[8 * s + 1]), a1 = cvtpk(acc[8 * s + 2], acc[8 * s + 3]);
    unsigned b0 = cvtpk(acc[8 * s + 4], acc[8 * s + 5]), b1 = cvtpk(acc[8 * s + 6], acc[8 * s + 7]);
    auto r0 = __builtin_amdgcn_permlane32_swap(a0, b0, false, false); auto r1 = __builtin_amdgcn_permlane32_swap(a1, b1, false, false);
    u32x4 o = {r0[0], r1[0], r0[1], r1[1]};
    *(u32x4*)(qf + (2 * c + s) * 1024 + lane * 16) = o;
  }
}

template <bool NT>
__global__ __launch_bounds__(512, 2) void k_attn(Params p) {
  extern __shared__ __attribute__((aligned(16))) char smem[];
  const unsigned tid = threadIdx.x, lane = tid & 63, w = rfl(tid >> 6);
  const unsigned wg = blockIdx.x, kvh = wg & 7, j = wg >> 3, br = j >> 4, b = (j >> 1) & 7, sp = j & 1;
  const unsigned L0 = (unsigned)(uintptr_t)(LAS char*)smem;
  constexpr int NST = 3, NTILE = 16;
  rsrc_t rk = mk_rsrc(p.k[br], NB * HKV * SK * DH * 2), rv = mk_rsrc(p.v[br], NB * HKV * SK * DH * 2);
  const unsigned lr = lane >> 4, fl = (lr << 2) | (w & 3), chs = (lane & 15) ^ fl;
  const unsigned vo = (4 * w + lr) * 256 + chs * 16;
  const unsigned gb = ((b * HKV + kvh) * SK + sp * 1024) * 256;
  auto issue = [&](int t) {
    const unsigned slot = L0 + (t % NST) * 32768 + w * 1024, so = gb + t * 16384;
    dma16<NT>(rk, vo, so, slot); dma16<NT>(rk, vo, so + 8192, slot + 8192);
    dma16<NT>(rv, vo, so, slot + 16384); dma16<NT>(rv, vo, so + 8192, slot + 16384 + 8192);
  };
  const unsigned head = w >> 1, kh = w & 1;
  const unsigned grp = (br * 8 + b) * 8 + kvh;
  bf16x8 qf[8];
  { const char* q = p.ws + WS_QF + ((size_t)grp * 4 + head) * 8192 + lane * 16;
#pragma unroll
    for (int s = 0; s < 4; ++s) asm volatile("global_load_dwordx4 %0, %1, off offset:%2" : "=v"(qf[s]) : "v"(q), "i"(s * 1024) : "memory");
    q += 4096;
#pragma unroll
    for (int s = 0; s < 4; ++s) asm volatile("global_load_dwordx4 %0, %1, off offset:%2" : "=v"(qf[4 + s]) : "v"(q), "i"(s * 1024) : "memory"); }
  for (int t = 0; t < NST - 1; ++t) issue(t);
  asm volatile("s_waitcnt vmcnt(%8)" : "+v"(qf[0]), "+v"(qf[1]), "+v"(qf[2]), "+v"(qf[3]), "+v"(qf[4]), "+v"(qf[5]), "+v"(qf[6]), "+v"(qf[7]) : "i"(4 * (NST - 1)) : "memory");
  const unsigned h = lane >> 5, r = lane & 31, blk = (lane >> 4) & 1, q4 = (lane & 15) >> 2, pp = lane & 3;
  const unsigned K0 = 256 * (32 * kh + r) + 16 * (h ^ fsw(r));
  const unsigned V0 = 16384 + 256 * (32 * kh + 4 * h + q4) + 64 * q4 + 16 * ((2 * blk + (pp >> 1)) ^ h) + 8 * (pp & 1);
  f32x16 o[4] = {};
  float m = -__builtin_inff(), l = 0.f;
  for (int t = 0; t < NTILE; ++t) {
    if (t + NST - 2 < NTILE) WAIT_VM(4 * (NST - 2)); else WAIT_VM(0);
    BARRIER();
    if (t + NST - 1 < NTILE) issue(t + NST - 1);
    const unsigned slot = L0 + (t % NST) * 32768;
    f32x16 s = {};
#pragma unroll
    for (int k = 0; k < 8; ++k) s = MFMA(lds_b128(slot + (K0 ^ (32 * k))), qf[k], s);
    float mx = s[0];
#pragma unroll
    for (int i = 1; i < 16; ++i) mx = fmaxf(mx, s[i]);
    mx = swapmax(mx);
    if (__any(mx > m)) {
      const float mn = fmaxf(m, mx), al = __builtin_amdgcn_exp2f((m - mn) * SCL);
      m = mn; l *= al;
#pragma unroll
      for (int c = 0; c < 4; ++c) o[c] *= al;
    }
    const float nm = -m * SCL;
#pragma unroll
    for (int i = 0; i < 16; ++i) { s[i] = __builtin_amdgcn_exp2f(fmaf(s[i], SCL, nm)); l += s[i]; }
    bf16x8 pf[2];
#pragma unroll
    for (int ks = 0; ks < 2; ++ks) {
      u32x4 u = {cvtpk(s[8 * ks], s[8 * ks + 1]), cvtpk(s[8 * ks + 2], s[8 * ks + 3]), cvtpk(s[8 * ks + 4], s[8 * ks + 5]), cvtpk(s[8 * ks + 6], s[8 * ks + 7])};
      pf[ks] = __builtin_bit_cast(bf16x8, u);
    }
#pragma unroll
    for (int c = 0; c < 4; ++c)
#pragma unroll
      for (int ks = 0; ks < 2; ++ks) {
        s16x4 a0 = lds_tr(slot + (V0 ^ (64 * c)) + 4096 * ks), a1 = lds_tr(slot + (V0 ^ (64 * c) ^ 32) + 2048 + 4096 * ks);
        o[c] = MFMA(cat(a0, a1), pf[ks], o[c]);
      }
  }
  l = swapadd(l);
  BARRIER();
  float* ml = (float*)smem;
  f32x4* ob = (f32x4*)(smem + 4096);
  if (h == 0) { ml[(w * 32 + r) * 2] = m; ml[(w * 32 + r) * 2 + 1] = l; }
  BARRIER();
  const float m2 = ml[((w ^ 1) * 32 + r) * 2], l2 = ml[((w ^ 1) * 32 + r) * 2 + 1];
  const float M = fmaxf(m, m2), a1 = __builtin_amdgcn_exp2f((m - M) * SCL), a2 = __builtin_amdgcn_exp2f((m2 - M) * SCL);
  const float L = l * a1 + l2 * a2;
  if (kh == 1) {
#pragma unroll
    for (int c = 0; c < 4; ++c)
#pragma unroll
      for (int g = 0; g < 4; ++g) { f32x4 v = {o[c][4 * g] * a1, o[c][4 * g + 1] * a1, o[c][4 * g + 2] * a1, o[c][4 * g + 3] * a1}; ob[((head * 4 + c) * 4 + g) * 64 + lane] = v; }
  }
  BARRIER();
  if (kh == 0) {
    f32x4* po = (f32x4*)(p.ws + WS_PO + (((size_t)grp * 2 + sp) * 4 + head) * 16384);
#pragma unroll
    for (int c = 0; c < 4; ++c)
#pragma unroll
      for (int g = 0; g < 4; ++g) { f32x4 v = ob[((head * 4 + c) * 4 + g) * 64 + lane];
        v[0] += o[c][4 * g] * a1; v[1] += o[c][4 * g + 1] * a1; v[2] += o[c][4 * g + 2] * a1; v[3] += o[c][4 * g + 3] * a1;
        po[(c * 4 + g) * 64 + lane] = v; }
    if (h == 0) { f32x2 v = {M, L}; *(f32x2*)(p.ws + WS_ML + ((((size_t)grp * 2 + sp) * 4 + head) * 32 + r) * 8) = v; }
  }
}

__global__ __launch_bounds__(512, 2) void k_oproj(Params p) {
  extern __shared__ __attribute__((aligned(16))) char smem[];
  const unsigned tid = threadIdx.x, lane = tid & 63, w = rfl(tid >> 6);
  const unsigned wg = blockIdx.x, kvh = wg & 7, j = wg >> 3, br = j >> 4, b = (j >> 1) & 7, hp = j & 1;
  const unsigned L0 = (unsigned)(uintptr_t)(LAS char*)smem;
  constexpr unsigned OFO = 0, WOFF = 16384; constexpr int NST = 2;
  rsrc_t rw = mk_rsrc(p.wo[br], HQ * DH * DM * 2);
  const unsigned lr = lane >> 4, fl = (lr << 2) | (w & 3), chs = (lane & 15) ^ fl;
  const unsigned vw = (4 * w + lr) * 1024 + chs * 16;
  const unsigned rowb = (kvh * 512 + hp * 256) * 1024;
  auto issue = [&](int i) {
    const unsigned kc = (i + b) & 3, slot = L0 + WOFF + (i % NST) * 65536 + w * 1024;
#pragma unroll
    for (int nc = 0; nc < 4; ++nc)
#pragma unroll
      for (int jj = 0; jj < 2; ++jj)
        dma16<false>(rw, vw, rowb + (kc * 64 + 32 * jj) * 1024 + nc * 256, slot + nc * 16384 + jj * 8192);
  };
  issue(0); issue(1);
  const unsigned grp = (br * 8 + b) * 8 + kvh;
  const unsigned h = lane >> 5, r = lane & 31, blk = (lane >> 4) & 1, q4 = (lane & 15) >> 2, pp = lane & 3;
  { const unsigned hh = w >> 2, c = w & 3, head = hp * 2 + hh;
    const f32x4* p0 = (const f32x4*)(p.ws + WS_PO + (((size_t)grp * 2 + 0) * 4 + head) * 16384) + c * 256 + lane;
    const f32x4* p1 = (const f32x4*)(p.ws + WS_PO + (((size_t)grp * 2 + 1) * 4 + head) * 16384) + c * 256 + lane;
    const f32x2 s0 = *(const f32x2*)(p.ws + WS_ML + ((((size_t)grp * 2 + 0) * 4 + head) * 32 + r) * 8);
    const f32x2 s1 = *(const f32x2*)(p.ws + WS_ML + ((((size_t)grp * 2 + 1) * 4 + head) * 32 + r) * 8);
    const float M = fmaxf(s0[0], s1[0]); float a0 = __builtin_amdgcn_exp2f((s0[0] - M) * SCL), a1 = __builtin_amdgcn_exp2f((s1[0] - M) * SCL);
    const float inv = 1.0f / (s0[1] * a0 + s1[1] * a1); a0 *= inv; a1 *= inv;
    unsigned pk[8];
#pragma unroll
    for (int g = 0; g < 4; ++g) { f32x4 u = p0[g * 64], v = p1[g * 64];
      pk[2 * g] = cvtpk(u[0] * a0 + v[0] * a1, u[1] * a0 + v[1] * a1); pk[2 * g + 1] = cvtpk(u[2] * a0 + v[2] * a1, u[3] * a0 + v[3] * a1); }
#pragma unroll
    for (int s = 0; s < 2; ++s) { u32x4 u = {pk[4 * s], pk[4 * s + 1], pk[4 * s + 2], pk[4 * s + 3]};
      *(LAS u32x4*)(uintptr_t)(L0 + OFO + ((hh * 4 + c) * 2 + s) * 1024 + lane * 16) = u; }
  }
  BARRIER();
  bf16x8 of[16];
#pragma unroll
  for (int i = 0; i < 16; ++i) of[i] = lds_b128(L0 + OFO + i * 1024 + lane * 16);
  const unsigned nc = w >> 1, cb0 = 2 * (w & 1);
  const unsigned W0 = nc * 16384 + 256 * (4 * h + q4) + 64 * q4 + 16 * ((2 * blk + (pp >> 1)) ^ h) + 8 * (pp & 1);
  f32x16 acc[2] = {};
#pragma unroll
  for (int i = 0; i < 4; ++i) {
    if (i < 3) WAIT_VM(8); else WAIT_VM(0);
    BARRIER();
    const unsigned slot = L0 + WOFF + (i % NST) * 65536;
    const unsigned kc = (i + b) & 3;
#pragma unroll
    for (int kk = 0; kk < 4; ++kk) {
      if (kc == (unsigned)kk) {
#pragma unroll
        for (int c2 = 0; c2 < 2; ++c2)
#pragma unroll
          for (int s = 0; s < 2; ++s)
#pragma unroll
            for (int ii = 0; ii < 2; ++ii) {
              const unsigned a = slot + (W0 ^ (64 * (cb0 + ii))) + 8192 * c2 + 4096 * s;
              s16x4 a0 = lds_tr(a), a1 = lds_tr((a ^ 32) + 2048);
              acc[ii] = MFMA(cat(a0, a1), of[kk * 4 + c2 * 2 + s], acc[ii]);
            }
      }
    }
    if (i + NST < 4) { BARRIER(); issue(i + NST); }
  }
  f32x4* yp = (f32x4*)(p.ws + WS_YP + ((size_t)(br * 8 + b) * 16 + kvh * 2 + hp) * 65536);
#pragma unroll
  for (int ii = 0; ii < 2; ++ii)
#pragma unroll
    for (int g = 0; g < 4; ++g) { f32x4 v = {acc[ii][4 * g], acc[ii][4 * g + 1], acc[ii][4 * g + 2], acc[ii][4 * g + 3]};
      yp[((nc * 4 + cb0 + ii) * 4 + g) * 64 + lane] = v; }
}

__global__ __launch_bounds__(256) void k_reduce(Params p) {
  const unsigned idx = blockIdx.x * 256 + threadIdx.x;
  const unsigned bb = idx >> 12, rem = idx & 4095, lane = rem & 63, g = (rem >> 6) & 3, nb = rem >> 8;
  const f32x4* yp = (const f32x4*)(p.ws + WS_YP + (size_t)bb * 16 * 65536) + rem;
  f32x4 a = {};
#pragma unroll
  for (int j = 0; j < 16; ++j) a += yp[j * 4096];
  const unsigned br = bb >> 3, b = bb & 7, pos = lane & 31, h = lane >> 5, n = 32 * nb + 8 * g + 4 * h;
  u32x2 o = {cvtpk(a[0], a[1]), cvtpk(a[2], a[3])};
  *(u32x2*)(p.out + ((size_t)(b * 32 + pos) * 1024 + br * 512 + n) * 2) = o;
}

extern "C" void kernel_launch(void* const* d_in, const int* in_sizes, int n_in, void* d_out, int out_size, void* d_ws, size_t ws_size, hipStream_t stream) {
  Params p;
  for (int i = 0; i < 2; ++i) {
    p.x[i] = (const char*)d_in[5 * i + 0]; p.k[i] = (const char*)d_in[5 * i + 1]; p.v[i] = (const char*)d_in[5 * i + 2];
    p.wq[i] = (const char*)d_in[5 * i + 3]; p.wo[i] = (const char*)d_in[5 * i + 4];
  }
  p.out = (char*)d_out; p.ws = (char*)d_ws;
  constexpr int S1 = 32768 + 4 * 32768, S2 = 3 * 32768, S3 = 16384 + 2 * 65536;
  hipFuncSetAttribute((const void*)k_qproj, hipFuncAttributeMaxDynamicSharedMemorySize, S1);
  hipFuncSetAttribute((const void*)k_attn<true>, hipFuncAttributeMaxDynamicSharedMemorySize, S2);
  hipFuncSetAttribute((const void*)k_oproj, hipFuncAttributeMaxDynamicSharedMemorySize, S3);
  hipLaunchKernelGGL(k_qproj, dim3(256), dim3(512), S1, stream, p);
  hipLaunchKernelGGL(k_attn<true>, dim3(256), dim3(512), S2, stream, p);
  hipLaunchKernelGGL(k_oproj, dim3(256), dim3(512), S3, stream, p);
  hipLaunchKernelGGL(k_reduce, dim3(256), dim3(256), 0, stream, p);
}
```

```cpp
#include <hip/hip_runtime.h>
#include <stdint.h>

typedef __attribute__((ext_vector_type(8))) short bf16x8;
typedef __attribute__((ext_vector_type(4))) short s16x4;
typedef __attribute__((ext_vector_type(16))) float f32x16;
typedef __attribute__((ext_vector_type(4))) float f32x4;
typedef __attribute__((ext_vector_type(2))) float f32x2;
typedef __attribute__((ext_vector_type(4))) unsigned u32x4;
typedef __attribute__((ext_vector_type(2))) unsigned u32x2;
typedef __attribute__((ext_vector_type(2))) __bf16 bf16x2;
#define LAS __attribute__((address_space(3)))
#define DI __device__ __forceinline__
#define MFMA(a, b, c) __builtin_amdgcn_mfma_f32_32x32x16_bf16((a), (b), (c), 0, 0, 0)

constexpr int NB = 8, SQ = 32, DM = 512, HQ = 32, HKV = 8, SK = 2048, DH = 128;
constexpr float SCL = 0.088388347648318447f * 1.4426950408889634f;

struct Params {
  const char* x[2]; const char* k[2]; const char* v[2]; const char* wq[2]; const char* wo[2];
  char* out; char* ws;
};
constexpr size_t WS_QF = 0;
constexpr size_t WS_PO = WS_QF + (4u << 20);
constexpr size_t WS_ML = WS_PO + (16u << 20);
constexpr size_t WS_YP = WS_ML + (1u << 20);

DI unsigned rfl(unsigned v) { return __builtin_amdgcn_readfirstlane(v); }
DI unsigned cvtpk(float lo, float hi) { f32x2 v = {lo, hi}; bf16x2 b = __builtin_convertvector(v, bf16x2); return __builtin_bit_cast(unsigned, b); }
DI float swapmax(float x) { auto r = __builtin_amdgcn_permlane32_swap(__float_as_uint(x), __float_as_uint(x), false, false); return fmaxf(__uint_as_float(r[0]), __uint_as_float(r[1])); }
DI float swapadd(float x) { auto r = __builtin_amdgcn_permlane32_swap(__float_as_uint(x), __float_as_uint(x), false, false); return __uint_as_float(r[0]) + __uint_as_float(r[1]); }

typedef __amdgpu_buffer_rsrc_t rsrc_t;
DI rsrc_t mk_rsrc(const void* p, unsigned bytes) { return __builtin_amdgcn_make_buffer_rsrc((void*)p, 0, bytes, 0x00020000); }
template <bool NT> DI void dma16(rsrc_t rs, unsigned voff, unsigned soff, unsigned ldsdst) {
  if constexpr (NT) asm volatile("s_mov_b32 m0, %0\n\ts_nop 0\n\tbuffer_load_dwordx4 %1, %2, %3 offen nt lds" :: "s"(ldsdst), "v"(voff), "s"(rs), "s"(soff) : "memory");
  else              asm volatile("s_mov_b32 m0, %0\n\ts_nop 0\n\tbuffer_load_dwordx4 %1, %2, %3 offen lds" :: "s"(ldsdst), "v"(voff), "s"(rs), "s"(soff) : "memory");
}
#define WAIT_VM(n) asm volatile("s_waitcnt vmcnt(%0)" :: "i"(n) : "memory")
#define BARRIER() do { asm volatile("s_waitcnt lgkmcnt(0)\n\ts_barrier" ::: "memory"); } while (0)

DI bf16x8 lds_b128(unsigned addr) { return *(const LAS bf16x8*)(uintptr_t)addr; }
DI s16x4 lds_tr(unsigned addr) { return __builtin_amdgcn_ds_read_tr16_b64_v4i16((LAS s16x4*)(uintptr_t)addr); }
DI bf16x8 cat(s16x4 a, s16x4 b) { return __builtin_shufflevector(a, b, 0, 1, 2, 3, 4, 5, 6, 7); }
DI unsigned fsw(unsigned row) { return ((row & 3) << 2) | ((row >> 2) & 3); }

__global__ __launch_bounds__(512, 2) void k_qproj(Params p) {
  extern __shared__ __attribute__((aligned(16))) char smem[];
  const unsigned tid = threadIdx.x, lane = tid & 63, w = rfl(tid >> 6);
  const unsigned wg = blockIdx.x, kvh = wg & 7, j = wg >> 3, br = j >> 4, b = (j >> 1) & 7, hp = j & 1;
  const unsigned L0 = (unsigned)(uintptr_t)(LAS char*)smem;
  constexpr unsigned XOFF = 0, WOFF = 32768; constexpr int NST = 4;
  rsrc_t rx = mk_rsrc(p.x[br], NB * SQ * DM * 2), rw = mk_rsrc(p.wq[br], DM * HQ * DH * 2);
  const unsigned lr = lane >> 4, fl = (lr << 2) | (w & 3), chs = (lane & 15) ^ fl;
  { const unsigned vx = (4 * w + lr) * 1024 + chs * 16;
    for (int kt = 0; kt < 4; ++kt) dma16<true>(rx, vx, b * (SQ * DM * 2) + kt * 256, L0 + XOFF + kt * 8192 + w * 1024); }
  const unsigned vw = (4 * w + lr) * 8192 + chs * 16;
  const unsigned colb = (kvh * 512 + hp * 256) * 2;
  auto issue = [&](int i) {
    const unsigned kc = (i + b) & 7, slot = L0 + WOFF + (i % NST) * 32768 + w * 1024;
#pragma unroll
    for (int hh = 0; hh < 2; ++hh)
#pragma unroll
      for (int jj = 0; jj < 2; ++jj)
        dma16<true>(rw, vw, (kc * 64 + 32 * jj) * 8192 + colb + hh * 256, slot + hh * 16384 + jj * 8192);
  };
  for (int i = 0; i < NST - 1; ++i) issue(i);
  const unsigned h = lane >> 5, r = lane & 31, blk = (lane >> 4) & 1, q = (lane & 15) >> 2, pp = lane & 3;
  const unsigned hh = w >> 2, c = w & 3;
  const unsigned A0 = 256 * (8 * h + q) + 64 * (c ^ q) + 16 * ((2 * blk + (pp >> 1)) ^ (2 * h)) + 8 * (pp & 1) + hh * 16384;
  const unsigned X0 = 256 * r + 16 * (h ^ fsw(r));
  f32x16 acc = {};
  for (int i = 0; i < 8; ++i) {
    if (i + NST - 2 < 8) WAIT_VM(4 * (NST - 2)); else WAIT_VM(0);
    BARRIER();
    if (i + NST - 1 < 8) issue(i + NST - 1);
    const unsigned kc = (i + b) & 7, slot = L0 + WOFF + (i % NST) * 32768;
    const unsigned xb = L0 + XOFF + (kc >> 1) * 8192;
#pragma unroll
    for (int ks = 0; ks < 4; ++ks) {
      s16x4 a0 = lds_tr(slot + A0 + 4096 * ks), a1 = lds_tr(slot + (A0 ^ 16) + 1024 + 4096 * ks);
      bf16x8 xf = lds_b128(xb + (X0 ^ (16 * (8 * (kc & 1) + 2 * ks))));
      acc = MFMA(cat(a0, a1), xf, acc);
    }
  }
  char* qf = p.ws + WS_QF + ((size_t)((br * 8 + b) * 8 + kvh) * 4 + hp * 2 + hh) * 8192;
#pragma unroll
  for (int s = 0; s < 2; ++s) {
    unsigned a0 = cvtpk(acc[8 * s + 0], acc[8 * s + 1]), a1 = cvtpk(acc[8 * s + 2], acc[8 * s + 3]);
    unsigned b0 = cvtpk(acc[8 * s + 4], acc[8 * s + 5]), b1 = cvtpk(acc[8 * s + 6], acc[8 * s + 7]);
    auto r0 = __builtin_amdgcn_permlane32_swap(a0, b0, false, false); auto r1 = __builtin_amdgcn_permlane32_swap(a1, b1, false, false);
    u32x4 o = {r0[0], r1[0], r0[1], r1[1]};
    *(u32x4*)(qf + (2 * c + s) * 1024 + lane * 16) = o;
  }
}

template <bool NT>
__global__ __launch_bounds__(512, 2) void k_attn(Params p) {
  extern __shared__ __attribute__((aligned(16))) char smem[];
  const unsigned tid = threadIdx.x, lane = tid & 63, w = rfl(tid >> 6);
  const unsigned wg = blockIdx.x, kvh = wg & 7, j = wg >> 3, br = j >> 4, b = (j >> 1) & 7, sp = j & 1;
  const unsigned L0 = (unsigned)(uintptr_t)(LAS char*)smem;
  constexpr int NST = 3, NTILE = 16;
  rsrc_t rk = mk_rsrc(p.k[br], NB * HKV * SK * DH * 2), rv = mk_rsrc(p.v[br], NB * HKV * SK * DH * 2);
  const unsigned lr = lane >> 4, fl = (lr << 2) | (w & 3), chs = (lane & 15) ^ fl;
  const unsigned vo = (4 * w + lr) * 256 + chs * 16;
  const unsigned gb = ((b * HKV + kvh) * SK + sp * 1024) * 256;
  auto issue = [&](int t) {
    const unsigned slot = L0 + (t % NST) * 32768 + w * 1024, so = gb + t * 16384;
    dma16<NT>(rk, vo, so, slot); dma16<NT>(rk, vo, so + 8192, slot + 8192);
    dma16<NT>(rv, vo, so, slot + 16384); dma16<NT>(rv, vo, so + 8192, slot + 16384 + 8192);
  };
  const unsigned head = w >> 1, kh = w & 1;
  const unsigned grp = (br * 8 + b) * 8 + kvh;
  bf16x8 qf[8];
  { const char* q = p.ws + WS_QF + ((size_t)grp * 4 + head) * 8192 + lane * 16;
#pragma unroll
    for (int s = 0; s < 4; ++s) asm volatile("global_load_dwordx4 %0, %1, off offset:%2" : "=v"(qf[s]) : "v"(q), "i"(s * 1024) : "memory");
    q += 4096;
#pragma unroll
    for (int s = 0; s < 4; ++s) asm volatile("global_load_dwordx4 %0, %1, off offset:%2" : "=v"(qf[4 + s]) : "v"(q), "i"(s * 1024) : "memory"); }
  for (int t = 0; t < NST - 1; ++t) issue(t);
  asm volatile("s_waitcnt vmcnt(%8)" : "+v"(qf[0]), "+v"(qf[1]), "+v"(qf[2]), "+v"(qf[3]), "+v"(qf[4]), "+v"(qf[5]), "+v"(qf[6]), "+v"(qf[7]) : "i"(4 * (NST - 1)) : "memory");
  const unsigned h = lane >> 5, r = lane & 31, blk = (lane >> 4) & 1, q4 = (lane & 15) >> 2, pp = lane & 3;
  const unsigned K0 = 256 * (32 * kh + r) + 16 * (h ^ fsw(r));
  const unsigned V0 = 16384 + 256 * (32 * kh + 4 * h + q4) + 64 * q4 + 16 * ((2 * blk + (pp >> 1)) ^ h) + 8 * (pp & 1);
  f32x16 o[4] = {};
  float m = -__builtin_inff(), l = 0.f;
  for (int t = 0; t < NTILE; ++t) {
    if (t + NST - 2 < NTILE) WAIT_VM(4 * (NST - 2)); else WAIT_VM(0);
    BARRIER();
    if (t + NST - 1 < NTILE) issue(t + NST - 1);
    const unsigned slot = L0 + (t % NST) * 32768;
    bf16x8 kf[8];
#pragma unroll
    for (int k = 0; k < 8; ++k) kf[k] = lds_b128(slot + (K0 ^ (32 * k)));
    __builtin_amdgcn_sched_barrier(0);
    f32x16 s = {};
#pragma unroll
    for (int k = 0; k < 8; ++k) s = MFMA(kf[k], qf[k], s);
    __builtin_amdgcn_sched_barrier(0);
    s16x4 va[4][2], vb[4][2];
#pragma unroll
    for (int c = 0; c < 4; ++c)
#pragma unroll
      for (int ks = 0; ks < 2; ++ks) { va[c][ks] = lds_tr(slot + (V0 ^ (64 * c)) + 4096 * ks); vb[c][ks] = lds_tr(slot + (V0 ^ (64 * c) ^ 32) + 2048 + 4096 * ks); }
    __builtin_amdgcn_sched_barrier(0);
    float mx = fmaxf(fmaxf(s[0], s[1]), s[2]);
#pragma unroll
    for (int i = 3; i < 15; i += 2) mx = fmaxf(fmaxf(mx, s[i]), s[i + 1]);
    mx = fmaxf(mx, s[15]);
    mx = swapmax(mx);
    if (__any(mx > m)) {
      const float mn = fmaxf(m, mx), al = __builtin_amdgcn_exp2f((m - mn) * SCL);
      m = mn; l *= al;
#pragma unroll
      for (int c = 0; c < 4; ++c) o[c] *= al;
    }
    const float nm = -m * SCL;
#pragma unroll
    for (int i = 0; i < 16; ++i) { s[i] = __builtin_amdgcn_exp2f(fmaf(s[i], SCL, nm)); l += s[i]; }
    bf16x8 pf[2];
#pragma unroll
    for (int ks = 0; ks < 2; ++ks) {
      u32x4 u = {cvtpk(s[8 * ks], s[8 * ks + 1]), cvtpk(s[8 * ks + 2], s[8 * ks + 3]), cvtpk(s[8 * ks + 4], s[8 * ks + 5]), cvtpk(s[8 * ks + 6], s[8 * ks + 7])};
      pf[ks] = __builtin_bit_cast(bf16x8, u);
    }
#pragma unroll
    for (int ks = 0; ks < 2; ++ks)
#pragma unroll
      for (int c = 0; c < 4; ++c) o[c] = MFMA(cat(va[c][ks], vb[c][ks]), pf[ks], o[c]);
  }
  l = swapadd(l);
  BARRIER();
  float* ml = (float*)smem;
  f32x4* ob = (f32x4*)(smem + 4096);
  if (h == 0) { ml[(w * 32 + r) * 2] = m; ml[(w * 32 + r) * 2 + 1] = l; }
  BARRIER();
  const float m2 = ml[((w ^ 1) * 32 + r) * 2], l2 = ml[((w ^ 1) * 32 + r) * 2 + 1];
  const float M = fmaxf(m, m2), a1 = __builtin_amdgcn_exp2f((m - M) * SCL), a2 = __builtin_amdgcn_exp2f((m2 - M) * SCL);
  const float L = l * a1 + l2 * a2;
  if (kh == 1) {
#pragma unroll
    for (int c = 0; c < 4; ++c)
#pragma unroll
      for (int g = 0; g < 4; ++g) { f32x4 v = {o[c][4 * g] * a1, o[c][4 * g + 1] * a1, o[c][4 * g + 2] * a1, o[c][4 * g + 3] * a1}; ob[((head * 4 + c) * 4 + g) * 64 + lane] = v; }
  }
  BARRIER();
  if (kh == 0) {
    f32x4* po = (f32x4*)(p.ws + WS_PO + (((size_t)grp * 2 + sp) * 4 + head) * 16384);
#pragma unroll
    for (int c = 0; c < 4; ++c)
#pragma unroll
      for (int g = 0; g < 4; ++g) { f32x4 v = ob[((head * 4 + c) * 4 + g) * 64 + lane];
        v[0] += o[c][4 * g] * a1; v[1] += o[c][4 * g + 1] * a1; v[2] += o[c][4 * g + 2] * a1; v[3] += o[c][4 * g + 3] * a1;
        po[(c * 4 + g) * 64 + lane] = v; }
    if (h == 0) { f32x2 v = {M, L}; *(f32x2*)(p.ws + WS_ML + ((((size_t)grp * 2 + sp) * 4 + head) * 32 + r) * 8) = v; }
  }
}

__global__ __launch_bounds__(512, 2) void k_oproj(Params p) {
  extern __shared__ __attribute__((aligned(16))) char smem[];
  const unsigned tid = threadIdx.x, lane = tid & 63, w = rfl(tid >> 6);
  const unsigned wg = blockIdx.x, kvh = wg & 7, j = wg >> 3, br = j >> 4, b = (j >> 1) & 7, hp = j & 1;
  const unsigned L0 = (unsigned)(uintptr_t)(LAS char*)smem;
  constexpr unsigned OFO = 0, WOFF = 16384; constexpr int NST = 2;
  rsrc_t rw = mk_rsrc(p.wo[br], HQ * DH * DM * 2);
  const unsigned lr = lane >> 4, fl = (lr << 2) | (w & 3), chs = (lane & 15) ^ fl;
  const unsigned vw = (4 * w + lr) * 1024 + chs * 16;
  const unsigned rowb = (kvh * 512 + hp * 256) * 1024;
  auto issue = [&](int i) {
    const unsigned kc = (i + b) & 3, slot = L0 + WOFF + (i % NST) * 65536 + w * 1024;
#pragma unroll
    for (int nc = 0; nc < 4; ++nc)
#pragma unroll
      for (int jj = 0; jj < 2; ++jj)
        dma16<true>(rw, vw, rowb + (kc * 64 + 32 * jj) * 1024 + nc * 256, slot + nc * 16384 + jj * 8192);
  };
  issue(0); issue(1);
  const unsigned grp = (br * 8 + b) * 8 + kvh;
  const unsigned h = lane >> 5, r = lane & 31, blk = (lane >> 4) & 1, q4 = (lane & 15) >> 2, pp = lane & 3;
  { const unsigned hh = w >> 2, c = w & 3, head = hp * 2 + hh;
    const f32x4* p0 = (const f32x4*)(p.ws + WS_PO + (((size_t)grp * 2 + 0) * 4 + head) * 16384) + c * 256 + lane;
    const f32x4* p1 = (const f32x4*)(p.ws + WS_PO + (((size_t)grp * 2 + 1) * 4 + head) * 16384) + c * 256 + lane;
    const f32x2 s0 = *(const f32x2*)(p.ws + WS_ML + ((((size_t)grp * 2 + 0) * 4 + head) * 32 + r) * 8);
    const f32x2 s1 = *(const f32x2*)(p.ws + WS_ML + ((((size_t)grp * 2 + 1) * 4 + head) * 32 + r) * 8);
    const float M = fmaxf(s0[0], s1[0]); float a0 = __builtin_amdgcn_exp2f((s0[0] - M) * SCL), a1 = __builtin_amdgcn_exp2f((s1[0] - M) * SCL);
    const float inv = 1.0f / (s0[1] * a0 + s1[1] * a1); a0 *= inv; a1 *= inv;
    unsigned pk[8];
#pragma unroll
    for (int g = 0; g < 4; ++g) { f32x4 u = p0[g * 64], v = p1[g * 64];
      pk[2 * g] = cvtpk(u[0] * a0 + v[0] * a1, u[1] * a0 + v[1] * a1); pk[2 * g + 1] = cvtpk(u[2] * a0 + v[2] * a1, u[3] * a0 + v[3] * a1); }
#pragma unroll
    for (int s = 0; s < 2; ++s) { u32x4 u = {pk[4 * s], pk[4 * s + 1], pk[4 * s + 2], pk[4 * s + 3]};
      *(LAS u32x4*)(uintptr_t)(L0 + OFO + ((hh * 4 + c) * 2 + s) * 1024 + lane * 16) = u; }
  }
  BARRIER();
  bf16x8 of[16];
#pragma unroll
  for (int i = 0; i < 16; ++i) of[i] = lds_b128(L0 + OFO + i * 1024 + lane * 16);
  const unsigned nc = w >> 1, cb0 = 2 * (w & 1);
  const unsigned W0 = nc * 16384 + 256 * (4 * h + q4) + 64 * q4 + 16 * ((2 * blk + (pp >> 1)) ^ h) + 8 * (pp & 1);
  f32x16 acc[2] = {};
#pragma unroll
  for (int i = 0; i < 4; ++i) {
    if (i < 3) WAIT_VM(8); else WAIT_VM(0);
    BARRIER();
    const unsigned slot = L0 + WOFF + (i % NST) * 65536;
    const unsigned kc = (i + b) & 3;
#pragma unroll
    for (int kk = 0; kk < 4; ++kk) {
      if (kc == (unsigned)kk) {
#pragma unroll
        for (int c2 = 0; c2 < 2; ++c2)
#pragma unroll
          for (int s = 0; s < 2; ++s)
#pragma unroll
            for (int ii = 0; ii < 2; ++ii) {
              const unsigned a = slot + (W0 ^ (64 * (cb0 + ii))) + 8192 * c2 + 4096 * s;
              s16x4 a0 = lds_tr(a), a1 = lds_tr((a ^ 32) + 2048);
              acc[ii] = MFMA(cat(a0, a1), of[kk * 4 + c2 * 2 + s], acc[ii]);
            }
      }
    }
    if (i + NST < 4) { BARRIER(); issue(i + NST); }
  }
  f32x4* yp = (f32x4*)(p.ws + WS_YP + ((size_t)(br * 8 + b) * 16 + kvh * 2 + hp) * 65536);
#pragma unroll
  for (int ii = 0; ii < 2; ++ii)
#pragma unroll
    for (int g = 0; g < 4; ++g) { f32x4 v = {acc[ii][4 * g], acc[ii][4 * g + 1], acc[ii][4 * g + 2], acc[ii][4 * g + 3]};
      yp[((nc * 4 + cb0 + ii) * 4 + g) * 64 + lane] = v; }
}

__global__ __launch_bounds__(256) void k_reduce(Params p) {
  const unsigned idx = blockIdx.x * 256 + threadIdx.x;
  const unsigned bb = idx >> 12, rem = idx & 4095, lane = rem & 63, g = (rem >> 6) & 3, nb = rem >> 8;
  const f32x4* yp = (const f32x4*)(p.ws + WS_YP + (size_t)bb * 16 * 65536) + rem;
  f32x4 a = {};
#pragma unroll
  for (int j = 0; j < 16; ++j) a += yp[j * 4096];
  const unsigned br = bb >> 3, b = bb & 7, pos = lane & 31, h = lane >> 5, n = 32 * nb + 8 * g + 4 * h;
  u32x2 o = {cvtpk(a[0], a[1]), cvtpk(a[2], a[3])};
  *(u32x2*)(p.out + ((size_t)(b * 32 + pos) * 1024 + br * 512 + n) * 2) = o;
}

extern "C" void kernel_launch(void* const* d_in, const int* in_sizes, int n_in, void* d_out, int out_size, void* d_ws, size_t ws_size, hipStream_t stream) {
  Params p;
  for (int i = 0; i < 2; ++i) {
    p.x[i] = (const char*)d_in[5 * i + 0]; p.k[i] = (const char*)d_in[5 * i + 1]; p.v[i] = (const char*)d_in[5 * i + 2];
    p.wq[i] = (const char*)d_in[5 * i + 3]; p.wo[i] = (const char*)d_in[5 * i + 4];
  }
  p.out = (char*)d_out; p.ws = (char*)d_ws;
  constexpr int S1 = 32768 + 4 * 32768, S2 = 3 * 32768, S3 = 16384 + 2 * 65536;
  hipFuncSetAttribute((const void*)k_qproj, hipFuncAttributeMaxDynamicSharedMemorySize, S1);
  hipFuncSetAttribute((const void*)k_attn<true>, hipFuncAttributeMaxDynamicSharedMemorySize, S2);
  hipFuncSetAttribute((const void*)k_oproj, hipFuncAttributeMaxDynamicSharedMemorySize, S3);
  hipLaunchKernelGGL(k_qproj, dim3(256), dim3(512), S1, stream, p);
  hipLaunchKernelGGL(k_attn<true>, dim3(256), dim3(512), S2, stream, p);
  hipLaunchKernelGGL(k_oproj, dim3(256), dim3(512), S3, stream, p);
  hipLaunchKernelGGL(k_reduce, dim3(256), dim3(256), 0, stream, p);
}
```

```cpp
#include <hip/hip_runtime.h>
#include <stdint.h>

typedef __attribute__((ext_vector_type(8))) short bf16x8;
typedef __attribute__((ext_vector_type(4))) short s16x4;
typedef __attribute__((ext_vector_type(16))) float f32x16;
typedef __attribute__((ext_vector_type(4))) float f32x4;
typedef __attribute__((ext_vector_type(2))) float f32x2;
typedef __attribute__((ext_vector_type(4))) unsigned u32x4;
typedef __attribute__((ext_vector_type(2))) unsigned u32x2;
typedef __attribute__((ext_vector_type(2))) __bf16 bf16x2;
#define LAS __attribute__((address_space(3)))
#define GAS __attribute__((address_space(1)))
#define DI __device__ __forceinline__
#define MFMA(a, b, c) __builtin_amdgcn_mfma_f32_32x32x16_bf16((a), (b), (c), 0, 0, 0)

#define RT 0
constexpr int NB = 8, SQ = 32, DM = 512, HQ = 32, HKV = 8, SK = 2048, DH = 128;
constexpr float SCL = 0.088388347648318447f * 1.4426950408889634f;
constexpr float THR = 8.0f;
constexpr int ET = (32 - RT) / 2, NT = ET + RT;
constexpr unsigned RSZ = 32768;

struct Params {
  const char* x[2]; const char* k[2]; const char* v[2]; const char* wq[2]; const char* wo[2];
  char* out; char* ws;
};
constexpr size_t WS_SYNC = 0;
constexpr size_t WS_QX = 1u << 20;
constexpr size_t WS_OX = WS_QX + (4u << 20);
constexpr size_t WS_ML = WS_OX + (8u << 20);
constexpr size_t WS_YP = WS_ML + (1u << 20);
constexpr size_t WS_DBG = 128u << 20;

DI unsigned rfl(unsigned v) { return __builtin_amdgcn_readfirstlane(v); }
DI unsigned cvtpk(float lo, float hi) { f32x2 v = {lo, hi}; bf16x2 b = __builtin_convertvector(v, bf16x2); return __builtin_bit_cast(unsigned, b); }
DI float bflo(unsigned u) { return __uint_as_float(u << 16); }
DI float bfhi(unsigned u) { return __uint_as_float(u & 0xffff0000u); }
DI float swapmax(float x) { auto r = __builtin_amdgcn_permlane32_swap(__float_as_uint(x), __float_as_uint(x), false, false); return fmaxf(__uint_as_float(r[0]), __uint_as_float(r[1])); }
DI float swapadd(float x) { auto r = __builtin_amdgcn_permlane32_swap(__float_as_uint(x), __float_as_uint(x), false, false); return __uint_as_float(r[0]) + __uint_as_float(r[1]); }
DI float lo_half(float x) { auto r = __builtin_amdgcn_permlane32_swap(__float_as_uint(x), __float_as_uint(x), false, false); return __uint_as_float(r[0]); }
DI float hi_half(float x) { auto r = __builtin_amdgcn_permlane32_swap(__float_as_uint(x), __float_as_uint(x), false, false); return __uint_as_float(r[1]); }

typedef __amdgpu_buffer_rsrc_t rsrc_t;
DI rsrc_t mk_rsrc(const void* p, unsigned bytes) { return __builtin_amdgcn_make_buffer_rsrc((void*)p, 0, bytes, 0x00020000); }
template <bool NTP> DI void dma16(rsrc_t rs, unsigned voff, unsigned soff, unsigned ldsdst) {
  if constexpr (NTP) asm volatile("s_mov_b32 m0, %0\n\ts_nop 0\n\tbuffer_load_dwordx4 %1, %2, %3 offen nt lds" :: "s"(ldsdst), "v"(voff), "s"(rs), "s"(soff) : "memory");
  else               asm volatile("s_mov_b32 m0, %0\n\ts_nop 0\n\tbuffer_load_dwordx4 %1, %2, %3 offen lds" :: "s"(ldsdst), "v"(voff), "s"(rs), "s"(soff) : "memory");
}
#define WNT true
#define STAMP(i) do {} while (0)
#define WAIT_VM(n) asm volatile("s_waitcnt vmcnt(%0)" :: "i"(n) : "memory")
#define BARRIER() asm volatile("s_waitcnt lgkmcnt(0)\n\ts_barrier" ::: "memory")

DI bf16x8 lds_b128(unsigned addr) { return *(const LAS bf16x8*)(uintptr_t)addr; }
DI u32x4 lds_u128(unsigned addr) { return *(const LAS u32x4*)(uintptr_t)addr; }
DI void lds_w128(unsigned addr, u32x4 v) { *(LAS u32x4*)(uintptr_t)addr = v; }
DI s16x4 lds_tr(unsigned addr) { return __builtin_amdgcn_ds_read_tr16_b64_v4i16((LAS s16x4*)(uintptr_t)addr); }
DI bf16x8 cat(s16x4 a, s16x4 b) { return __builtin_shufflevector(a, b, 0, 1, 2, 3, 4, 5, 6, 7); }
DI void st_sc1(rsrc_t rs, unsigned off, u32x4 v) { __builtin_amdgcn_raw_buffer_store_b128(v, rs, off, 0, 16); }
DI u32x4 ld_sc1(rsrc_t rs, unsigned off) { return __builtin_amdgcn_raw_buffer_load_b128(rs, off, 0, 16); }
DI void st32_dev(void* p, unsigned v) { __hip_atomic_store((GAS unsigned*)p, v, __ATOMIC_RELAXED, __HIP_MEMORY_SCOPE_AGENT); }
DI unsigned ld32_dev(const void* p) { return __hip_atomic_load((GAS unsigned*)p, __ATOMIC_RELAXED, __HIP_MEMORY_SCOPE_AGENT); }
DI void wait_flags(const char* base, unsigned n, unsigned tag, unsigned lane) {
  const unsigned long long t0 = __builtin_amdgcn_s_memrealtime();
  for (;;) {
    const unsigned v = ld32_dev(base + 4 * (lane < n ? lane : 0));
    if (__all(v == tag)) break;
    if (__builtin_amdgcn_s_memrealtime() - t0 > 4000000ull) break;
    __builtin_amdgcn_s_sleep(1);
  }
  asm volatile("" ::: "memory");
}

__global__ __launch_bounds__(512, 2) void k_fused(Params p) {
  extern __shared__ __attribute__((aligned(16))) char smem[];
  const unsigned tid = threadIdx.x, lane = tid & 63, w = rfl(tid >> 6);
  const unsigned wg = blockIdx.x, kvh = wg & 7, j = wg >> 3, br = j >> 4, b = (j >> 1) & 7, sp = j & 1;
  const unsigned grp = br * 8 + b, mem = kvh * 2 + sp, pwg = wg ^ 8, pmem = mem ^ 1;
  const unsigned L0 = (unsigned)(uintptr_t)(LAS char*)smem;
  const bool dmaw = w < 4;
  STAMP(0);
  char* sync = p.ws + WS_SYNC + grp * 8192;
  unsigned tag = 0;
  if (!dmaw) tag = ld32_dev(sync) + 1u;

  rsrc_t rx = mk_rsrc(p.x[br], NB * SQ * DM * 2), rwq = mk_rsrc(p.wq[br], DM * HQ * DH * 2), rwo = mk_rsrc(p.wo[br], HQ * DH * DM * 2);
  rsrc_t rk = mk_rsrc(p.k[br], NB * HKV * SK * DH * 2), rv = mk_rsrc(p.v[br], NB * HKV * SK * DH * 2);
  rsrc_t rws = mk_rsrc(p.ws, 64u << 20);

  const unsigned w4 = w & 3;
  const unsigned drow = 8 * (w4 >> 1) + ((lane >> 2) & 7), dch = 8 * (w4 & 1) + 4 * (lane >> 5) + ((lane & 3) ^ ((drow >> 2) & 3));
  const unsigned voKV = drow * 256 + dch * 16, voWQ = drow * 8192 + dch * 16, voWO = drow * 1024 + dch * 16;
  const unsigned kvbase = (b * HKV + kvh) * SK * 256;
  auto kvslot = [&](int u) -> unsigned { return (u < 10 ? u % 5 : (u - 10) % 3) * RSZ; };
  auto issue_kv = [&](int u) {
    const unsigned gt = u < ET ? sp * ET + u : 2 * ET + (u - ET);
    const unsigned slot = L0 + kvslot(u) + w4 * 1024, so = kvbase + gt * 16384;
#pragma unroll
    for (int i = 0; i < 4; ++i) dma16<true>(rk, voKV, so + 4096 * i, slot + 4096 * i);
#pragma unroll
    for (int i = 0; i < 4; ++i) dma16<true>(rv, voKV, so + 4096 * i, slot + 16384 + 4096 * i);
  };
  const unsigned wqcol = (kvh * 512 + sp * 256) * 2;
  auto wqslot = [&](int i) -> unsigned { return (1 + (i + 1) % 3) * RSZ; };
  auto issue_wq = [&](int i) {
    const unsigned kc = (i + b) & 7, slot = L0 + wqslot(i) + w4 * 1024;
#pragma unroll
    for (int hh = 0; hh < 2; ++hh)
#pragma unroll
      for (int ii = 0; ii < 4; ++ii) dma16<WNT>(rwq, voWQ, (kc * 64 + 16 * ii) * 8192 + wqcol + hh * 256, slot + hh * 16384 + 4096 * ii);
  };
  const unsigned worow = kvh * 512 + sp * 256;
  auto issue_wo = [&](int i, unsigned slotoff) {
    const unsigned kc = ((i >> 1) + b) & 3, nh = i & 1, slot = L0 + slotoff + w4 * 1024;
#pragma unroll
    for (int tt = 0; tt < 2; ++tt)
#pragma unroll
      for (int ii = 0; ii < 4; ++ii) dma16<WNT>(rwo, voWO, (worow + kc * 64 + 16 * ii) * 1024 + (2 * nh + tt) * 256, slot + tt * 16384 + 4096 * ii);
  };

  const unsigned h = lane >> 5, r = lane & 31, blk = (lane >> 4) & 1, q4 = (lane & 15) >> 2, pp = lane & 3;
  const unsigned rr0 = 2048 * (r >> 3) + 64 * (r & 7) + 16 * ((0 + h) ^ ((r >> 2) & 3));
  const unsigned rr1 = 2048 * (r >> 3) + 64 * (r & 7) + 16 * ((2 + h) ^ ((r >> 2) & 3));
  const unsigned tp0 = 64 * (4 * h + q4) + 16 * ((2 * blk + (pp >> 1)) ^ (0 + h)) + 8 * (pp & 1);
  const unsigned tp1 = 2048 + 64 * (4 * h + q4) + 16 * ((2 * blk + (pp >> 1)) ^ (2 + h)) + 8 * (pp & 1);

  bf16x8 qf[8];
  {
    if (dmaw) {
#pragma unroll
      for (int kt = 0; kt < 4; ++kt)
#pragma unroll
        for (int ii = 0; ii < 2; ++ii) dma16<WNT>(rx, voWO, b * (SQ * DM * 2) + kt * 256 + 16 * ii * 1024, L0 + 4 * RSZ + kt * 8192 + w4 * 1024 + 4096 * ii);
      issue_wq(0); issue_wq(1); issue_wq(2); issue_kv(0);
    }
    const unsigned hh = w >> 2, c = w & 3;
    const unsigned tn0 = 2048 * h + 64 * q4 + 16 * ((2 * blk + (pp >> 1)) ^ (2 * h)) + 8 * (pp & 1) + 512 * c + hh * 16384;
    const unsigned tn1 = 2048 * h + 256 + 64 * q4 + 16 * ((2 * blk + (pp >> 1)) ^ (2 * h + 1)) + 8 * (pp & 1) + 512 * c + hh * 16384;
    f32x16 acc = {};
#pragma unroll
    for (int i = 0; i < 8; ++i) {
      if (i == 0) WAIT_VM(24); else if (i < 3) WAIT_VM(16); else WAIT_VM(8);
      BARRIER();
      if (i == 0) STAMP(1);
      if (dmaw) { if (i >= 1 && i + 2 < 8) issue_wq(i + 2); if (i == 6) issue_kv(1); if (i == 7) issue_kv(2); }
      const unsigned kc = (i + b) & 7, slot = L0 + wqslot(i);
      const unsigned xb = L0 + 4 * RSZ + (kc >> 1) * 8192 + (kc & 1) * 1024;
      s16x4 a0[4], a1[4]; bf16x8 xf[4];
#pragma unroll
      for (int ks = 0; ks < 4; ++ks) { a0[ks] = lds_tr(slot + tn0 + 4096 * ks); a1[ks] = lds_tr(slot + tn1 + 4096 * ks); xf[ks] = lds_b128(xb + ((ks & 1) ? rr1 : rr0) + 512 * (ks >> 1)); }
#pragma unroll
      for (int ks = 0; ks < 4; ++ks) acc = MFMA(cat(a0[ks], a1[ks]), xf[ks], acc);
    }
    BARRIER();
    STAMP(2);
    if (dmaw) issue_kv(3);
    const unsigned QF = L0 + 4 * RSZ;
#pragma unroll
    for (int s = 0; s < 2; ++s) {
      unsigned a0 = cvtpk(acc[8 * s + 0] * SCL, acc[8 * s + 1] * SCL), a1 = cvtpk(acc[8 * s + 2] * SCL, acc[8 * s + 3] * SCL);
      unsigned b0 = cvtpk(acc[8 * s + 4] * SCL, acc[8 * s + 5] * SCL), b1 = cvtpk(acc[8 * s + 6] * SCL, acc[8 * s + 7] * SCL);
      auto r0 = __builtin_amdgcn_permlane32_swap(a0, b0, false, false); auto r1 = __builtin_amdgcn_permlane32_swap(a1, b1, false, false);
      u32x4 o = {r0[0], r1[0], r0[1], r1[1]};
      lds_w128(QF + (hh * 8 + 2 * c + s) * 1024 + lane * 16, o);
    }
    BARRIER();
    u32x4 qs[4];
    if (dmaw) {
#pragma unroll
      for (int s = 0; s < 8; ++s) qf[s] = lds_b128(QF + (((w >> 1) & 1) * 8 + s) * 1024 + lane * 16);
    } else {
#pragma unroll
      for (int s = 0; s < 4; ++s) qs[s] = lds_u128(QF + (w4 * 4 + s) * 1024 + lane * 16);
#pragma unroll
      for (int s = 0; s < 4; ++s) st_sc1(rws, WS_QX + wg * 16384 + (w4 * 4 + s) * 1024 + lane * 16, qs[s]);
    }
    BARRIER();
    if (dmaw) issue_kv(4);
    else WAIT_VM(0);
    BARRIER();
    if (w == 4) {
      if (lane == 0) st32_dev(sync + 1024 + 128 * mem, tag);
      STAMP(3);
      wait_flags(sync + 1024 + 128 * pmem, 1, tag, lane);
      STAMP(4);
    }
    BARRIER();
    if (!dmaw) {
#pragma unroll
      for (int s = 0; s < 8; ++s) qf[s] = __builtin_bit_cast(bf16x8, ld_sc1(rws, WS_QX + pwg * 16384 + (((w >> 1) & 1) * 8 + s) * 1024 + lane * 16));
      asm volatile("" : "+v"(qf[0]), "+v"(qf[1]), "+v"(qf[2]), "+v"(qf[3]), "+v"(qf[4]), "+v"(qf[5]), "+v"(qf[6]), "+v"(qf[7]));
    }
  }

  const unsigned kh = w & 1;
  const unsigned kb0 = L0 + 8192 * kh + rr0, kb1 = L0 + 8192 * kh + rr1, vb0 = L0 + 16384 + 8192 * kh + tp0, vb1 = L0 + 16384 + 8192 * kh + tp1;
  f32x16 o[4] = {};
  f32x16 negm = {};
  float m = 0.f, l = 0.f;
  u32x4 pk[8];
  for (int t = 0; t < NT; ++t) {
    if (t <= 12 && t < NT - 2) WAIT_VM(24); else if (t < NT - 1) WAIT_VM(8); else WAIT_VM(0);
    BARRIER();
    if (t == 0) STAMP(5);
    if (t == 8) STAMP(6);
    if (dmaw) {
      if (t >= 1 && t <= 8) issue_kv(t + 4);
      else if (t == 9) issue_wo(0, 3 * RSZ);
      else if (t == 10) issue_wo(1, 4 * RSZ);
      else if (t >= 11 && t + 2 < NT) issue_kv(t + 2);
    }
    if (dmaw || t < ET) {
      const unsigned so = kvslot(t);
      const unsigned k0 = kb0 + so, k1 = kb1 + so, v0 = vb0 + so, v1 = vb1 + so;
      bf16x8 kf[8];
#pragma unroll
      for (int k = 0; k < 8; ++k) kf[k] = lds_b128(((k & 1) ? k1 : k0) + 512 * (k >> 1));
      __builtin_amdgcn_sched_barrier(0);
      f32x16 s = MFMA(kf[0], qf[0], negm);
#pragma unroll
      for (int k = 1; k < 8; ++k) s = MFMA(kf[k], qf[k], s);
      s16x4 va[4][2], vc[4][2];
#pragma unroll
      for (int ks = 0; ks < 2; ++ks)
#pragma unroll
        for (int c = 0; c < 4; ++c) { va[c][ks] = lds_tr(v0 + 4096 * ks + 512 * c); vc[c][ks] = lds_tr(v1 + 4096 * ks + 512 * c); }
      __builtin_amdgcn_sched_barrier(0);
      float mx = fmaxf(fmaxf(s[0], s[1]), s[2]);
#pragma unroll
      for (int i = 3; i < 15; i += 2) mx = fmaxf(fmaxf(mx, s[i]), s[i + 1]);
      mx = fmaxf(mx, s[15]);
      mx = swapmax(mx);
      if (__builtin_expect(t == 0 || __any(mx > THR), 0)) {
        const float dl = (t == 0) ? mx : fmaxf(mx, 0.f), al = (t == 0) ? 0.f : __builtin_amdgcn_exp2f(-dl);
        m += dl; l *= al;
#pragma unroll
        for (int i = 0; i < 16; ++i) s[i] -= dl;
        asm volatile("s_nop 1\n\tv_mfma_f32_32x32x2_f32 %0, %1, %2, 0\n\ts_nop 7\n\ts_nop 7\n\ts_nop 3" : "+v"(negm) : "v"(h ? 0.f : 1.f), "v"(-m));
#pragma unroll
        for (int c = 0; c < 4; ++c) o[c] *= al;
      }
#pragma unroll
      for (int i = 0; i < 16; ++i) s[i] = __builtin_amdgcn_exp2f(s[i]);
      bf16x8 pf[2];
#pragma unroll
      for (int ks = 0; ks < 2; ++ks) {
        u32x4 u = {cvtpk(s[8 * ks], s[8 * ks + 1]), cvtpk(s[8 * ks + 2], s[8 * ks + 3]), cvtpk(s[8 * ks + 4], s[8 * ks + 5]), cvtpk(s[8 * ks + 6], s[8 * ks + 7])};
        pf[ks] = __builtin_bit_cast(bf16x8, u);
      }
#pragma unroll
      for (int ks = 0; ks < 2; ++ks)
#pragma unroll
        for (int c = 0; c < 4; ++c) o[c] = MFMA(cat(va[c][ks], vc[c][ks]), pf[ks], o[c]);
      float l2 = 0.f;
#pragma unroll
      for (int i = 0; i < 16; i += 2) { l += s[i]; l2 += s[i + 1]; }
      l += l2;
    }
  }
  STAMP(7);

  constexpr unsigned S3 = ((NT - 3) < 10 ? (NT - 3) % 5 : (NT - 3 - 10) % 3) * RSZ;
  constexpr unsigned PS = ((NT - 2) < 10 ? (NT - 2) % 5 : (NT - 2 - 10) % 3) * RSZ;
  constexpr unsigned QS = ((NT - 1) < 10 ? (NT - 1) % 5 : (NT - 1 - 10) % 3) * RSZ;
  l = swapadd(l);
  {
    const float inv = 1.0f / l;
#pragma unroll
    for (int c = 0; c < 4; ++c)
#pragma unroll
      for (int s = 0; s < 2; ++s) {
        u32x4 u = {cvtpk(o[c][8 * s] * inv, o[c][8 * s + 1] * inv), cvtpk(o[c][8 * s + 2] * inv, o[c][8 * s + 3] * inv),
                   cvtpk(o[c][8 * s + 4] * inv, o[c][8 * s + 5] * inv), cvtpk(o[c][8 * s + 6] * inv, o[c][8 * s + 7] * inv)};
        pk[c * 2 + s] = u;
      }
  }
  const float mlv = h ? l : m;
  if (dmaw) {
#pragma unroll
    for (int i = 0; i < 8; ++i) lds_w128(L0 + PS + w4 * 8192 + i * 1024 + lane * 16, pk[i]);
    *(LAS float*)(uintptr_t)(L0 + S3 + w4 * 256 + lane * 4) = mlv;
  } else {
#pragma unroll
    for (int i = 0; i < 8; ++i) st_sc1(rws, WS_OX + wg * 32768 + w4 * 8192 + i * 1024 + lane * 16, pk[i]);
    __builtin_amdgcn_raw_buffer_store_b32(__float_as_uint(mlv), rws, WS_ML + wg * 1024 + w4 * 256 + lane * 4, 0, 16);
    WAIT_VM(0);
  }
  BARRIER();
  if (dmaw) issue_wo(2, QS);
  if (w == 4) {
    if (lane == 0) st32_dev(sync + 4096 + 128 * mem, tag);
    STAMP(8);
    wait_flags(sync + 4096 + 128 * pmem, 1, tag, lane);
    STAMP(9);
  }
  BARRIER();
  if (!dmaw) {
    const unsigned hh = (w >> 1) & 1;
    u32x4 ga[4], gb[4], la[4], lb[4];
#pragma unroll
    for (int i = 0; i < 4; ++i) {
      ga[i] = ld_sc1(rws, WS_OX + pwg * 32768 + (hh * 2 + 0) * 8192 + (4 * kh + i) * 1024 + lane * 16);
      gb[i] = ld_sc1(rws, WS_OX + pwg * 32768 + (hh * 2 + 1) * 8192 + (4 * kh + i) * 1024 + lane * 16);
    }
    const float g0 = __uint_as_float(__builtin_amdgcn_raw_buffer_load_b32(rws, WS_ML + pwg * 1024 + (hh * 2 + 0) * 256 + lane * 4, 0, 16));
    const float g1 = __uint_as_float(__builtin_amdgcn_raw_buffer_load_b32(rws, WS_ML + pwg * 1024 + (hh * 2 + 1) * 256 + lane * 4, 0, 16));
#pragma unroll
    for (int i = 0; i < 4; ++i) {
      la[i] = lds_u128(L0 + PS + (hh * 2 + 0) * 8192 + (4 * kh + i) * 1024 + lane * 16);
      lb[i] = lds_u128(L0 + PS + (hh * 2 + 1) * 8192 + (4 * kh + i) * 1024 + lane * 16);
    }
    const float e0 = *(const LAS float*)(uintptr_t)(L0 + S3 + (hh * 2 + 0) * 256 + lane * 4);
    const float e1 = *(const LAS float*)(uintptr_t)(L0 + S3 + (hh * 2 + 1) * 256 + lane * 4);
    const float m0 = lo_half(e0), l0 = hi_half(e0), m1 = lo_half(e1), l1 = hi_half(e1);
    const float m2 = lo_half(g0), l2 = hi_half(g0), m3 = lo_half(g1), l3 = hi_half(g1);
    const float M = fmaxf(fmaxf(m0, m1), fmaxf(m2, m3));
    float w0 = l0 * __builtin_amdgcn_exp2f(m0 - M), w1 = l1 * __builtin_amdgcn_exp2f(m1 - M), w2 = l2 * __builtin_amdgcn_exp2f(m2 - M), w3 = l3 * __builtin_amdgcn_exp2f(m3 - M);
    const float inv = 1.0f / ((w0 + w1) + (w2 + w3));
    w0 *= inv; w1 *= inv; w2 *= inv; w3 *= inv;
#pragma unroll
    for (int i = 0; i < 4; ++i) {
      u32x4 u;
#pragma unroll
      for (int e = 0; e < 4; ++e) {
        const float lo = bflo(la[i][e]) * w0 + bflo(lb[i][e]) * w1 + bflo(ga[i][e]) * w2 + bflo(gb[i][e]) * w3;
        const float hi = bfhi(la[i][e]) * w0 + bfhi(lb[i][e]) * w1 + bfhi(ga[i][e]) * w2 + bfhi(gb[i][e]) * w3;
        u[e] = cvtpk(lo, hi);
      }
      lds_w128(L0 + S3 + 1024 + (hh * 8 + 4 * kh + i) * 1024 + lane * 16, u);
    }
  }
  BARRIER();
  STAMP(10);
  if (dmaw) issue_wo(3, PS);
  f32x16 acc[2] = {};
  {
    const unsigned wt0 = (w >> 2) * 16384 + 512 * (w & 3) + tp0, wt1 = (w >> 2) * 16384 + 512 * (w & 3) + tp1;
#pragma unroll
    for (int i = 0; i < 8; ++i) {
      if (i == 0) WAIT_VM(16); else if (i < 5) WAIT_VM(16); else if (i == 5) WAIT_VM(16); else if (i == 6) WAIT_VM(8); else WAIT_VM(0);
      if (i > 0) BARRIER();
      const unsigned so = (i & 3) == 0 ? 3 * RSZ : (i & 3) == 1 ? 4 * RSZ : (i & 3) == 2 ? QS : PS;
      if (dmaw && i >= 1 && i + 3 < 8) { const unsigned sn = ((i + 3) & 3) == 0 ? 3 * RSZ : ((i + 3) & 3) == 1 ? 4 * RSZ : ((i + 3) & 3) == 2 ? QS : PS; issue_wo(i + 3, sn); }
      const unsigned kc = ((i >> 1) + b) & 3;
      const unsigned ofb = L0 + S3 + 1024 + kc * 4096 + lane * 16;
      s16x4 a0[4], a1[4]; bf16x8 of[4];
#pragma unroll
      for (int q = 0; q < 4; ++q) { a0[q] = lds_tr(L0 + so + wt0 + 4096 * q); a1[q] = lds_tr(L0 + so + wt1 + 4096 * q); of[q] = lds_b128(ofb + q * 1024); }
#pragma unroll
      for (int q = 0; q < 4; ++q) acc[i & 1] = MFMA(cat(a0[q], a1[q]), of[q], acc[i & 1]);
    }
  }
  STAMP(11);
#pragma unroll
  for (int nh = 0; nh < 2; ++nh)
#pragma unroll
    for (int g = 0; g < 4; ++g) {
      u32x4 v = {__float_as_uint(acc[nh][4 * g]), __float_as_uint(acc[nh][4 * g + 1]), __float_as_uint(acc[nh][4 * g + 2]), __float_as_uint(acc[nh][4 * g + 3])};
      st_sc1(rws, WS_YP + (grp * 16 + mem) * 65536 + ((nh * 8 + w) * 4 + g) * 1024 + lane * 16, v);
    }
  WAIT_VM(0);
  BARRIER();
  STAMP(12);
  if (w == 4) {
    if (lane == 0) st32_dev(sync + 128 + 4 * mem, tag);
    wait_flags(sync + 128, 16, tag, lane);
    if (lane == 0 && mem == 0) st32_dev(sync, tag);
    STAMP(13);
  }
  BARRIER();
  if (w < 4) {
    f32x4 a = {};
    u32x4 v[16];
#pragma unroll
    for (int i = 0; i < 16; ++i) v[i] = ld_sc1(rws, WS_YP + (grp * 16 + i) * 65536 + (mem * 4 + w) * 1024 + lane * 16);
#pragma unroll
    for (int i = 0; i < 16; ++i) { a[0] += __uint_as_float(v[i][0]); a[1] += __uint_as_float(v[i][1]); a[2] += __uint_as_float(v[i][2]); a[3] += __uint_as_float(v[i][3]); }
    const unsigned n = 32 * mem + 8 * w + 4 * h;
    u32x2 ov = {cvtpk(a[0], a[1]), cvtpk(a[2], a[3])};
    *(u32x2*)(p.out + ((size_t)(b * 32 + r) * 1024 + br * 512 + n) * 2) = ov;
  }
  STAMP(14);
}

extern "C" void kernel_launch(void* const* d_in, const int* in_sizes, int n_in, void* d_out, int out_size, void* d_ws, size_t ws_size, hipStream_t stream) {
  Params p;
  for (int i = 0; i < 2; ++i) {
    p.x[i] = (const char*)d_in[5 * i + 0]; p.k[i] = (const char*)d_in[5 * i + 1]; p.v[i] = (const char*)d_in[5 * i + 2];
    p.wq[i] = (const char*)d_in[5 * i + 3]; p.wo[i] = (const char*)d_in[5 * i + 4];
  }
  p.out = (char*)d_out; p.ws = (char*)d_ws;
  constexpr int SMEM = 5 * 32768;
  (void)hipFuncSetAttribute((const void*)k_fused, hipFuncAttributeMaxDynamicSharedMemorySize, SMEM);
  hipLaunchKernelGGL(k_fused, dim3(256), dim3(512), SMEM, stream, p);
}
```

```cpp
#include <hip/hip_runtime.h>
#include <stdint.h>
#include <type_traits>

typedef __attribute__((ext_vector_type(8))) short bf16x8;
typedef __attribute__((ext_vector_type(4))) short s16x4;
typedef __attribute__((ext_vector_type(16))) float f32x16;
typedef __attribute__((ext_vector_type(4))) float f32x4;
typedef __attribute__((ext_vector_type(2))) float f32x2;
typedef __attribute__((ext_vector_type(4))) unsigned u32x4;
typedef __attribute__((ext_vector_type(2))) unsigned u32x2;
typedef __attribute__((ext_vector_type(2))) __bf16 bf16x2;
#define LAS __attribute__((address_space(3)))
#define GAS __attribute__((address_space(1)))
#define DI __device__ __forceinline__
#define MFMA(a, b, c) __builtin_amdgcn_mfma_f32_32x32x16_bf16((a), (b), (c), 0, 0, 0)

constexpr int NB = 8, SQ = 32, DM = 512, HQ = 32, HKV = 8, SK = 2048, DH = 128;
constexpr float SCL = 0.088388347648318447f * 1.4426950408889634f;
constexpr float THR = 8.0f;
constexpr int NT = 32;
constexpr unsigned RSZ = 32768;

struct Params {
  const char* x[2]; const char* k[2]; const char* v[2]; const char* wq[2]; const char* wo[2];
  char* out; char* ws;
};
constexpr size_t WS_SYNC = 0;
constexpr size_t WS_YP = 1u << 20;
constexpr size_t WS_DBG = 128u << 20;

DI unsigned rfl(unsigned v) { return __builtin_amdgcn_readfirstlane(v); }
DI unsigned cvtpk(float lo, float hi) { f32x2 v = {lo, hi}; bf16x2 b = __builtin_convertvector(v, bf16x2); return __builtin_bit_cast(unsigned, b); }
DI float bflo(unsigned u) { return __uint_as_float(u << 16); }
DI float bfhi(unsigned u) { return __uint_as_float(u & 0xffff0000u); }
DI float swapmax(float x) { auto r = __builtin_amdgcn_permlane32_swap(__float_as_uint(x), __float_as_uint(x), false, false); return fmaxf(__uint_as_float(r[0]), __uint_as_float(r[1])); }
DI float swapadd(float x) { auto r = __builtin_amdgcn_permlane32_swap(__float_as_uint(x), __float_as_uint(x), false, false); return __uint_as_float(r[0]) + __uint_as_float(r[1]); }
DI float lo_half(float x) { auto r = __builtin_amdgcn_permlane32_swap(__float_as_uint(x), __float_as_uint(x), false, false); return __uint_as_float(r[0]); }
DI float hi_half(float x) { auto r = __builtin_amdgcn_permlane32_swap(__float_as_uint(x), __float_as_uint(x), false, false); return __uint_as_float(r[1]); }

typedef __amdgpu_buffer_rsrc_t rsrc_t;
DI rsrc_t mk_rsrc(const void* p, unsigned bytes) { return __builtin_amdgcn_make_buffer_rsrc((void*)p, 0, bytes, 0x00020000); }
template <bool NTP> DI void dma16(rsrc_t rs, unsigned voff, unsigned soff, unsigned ldsdst) {
  if constexpr (NTP) asm volatile("s_mov_b32 m0, %0\n\ts_nop 0\n\tbuffer_load_dwordx4 %1, %2, %3 offen nt lds" :: "s"(ldsdst), "v"(voff), "s"(rs), "s"(soff) : "memory");
  else               asm volatile("s_mov_b32 m0, %0\n\ts_nop 0\n\tbuffer_load_dwordx4 %1, %2, %3 offen lds" :: "s"(ldsdst), "v"(voff), "s"(rs), "s"(soff) : "memory");
}
#define KVNT false
#define WQNT true
#define WONT false
#define STAMP(i) do {} while (0)
#define STAMPC(i) do {} while (0)
#define WAIT_VM(n) asm volatile("s_waitcnt vmcnt(%0)" :: "i"(n) : "memory")
#define BARRIER() asm volatile("s_waitcnt lgkmcnt(0)\n\ts_barrier" ::: "memory")

DI bf16x8 lds_b128(unsigned addr) { return *(const LAS bf16x8*)(uintptr_t)addr; }
DI u32x4 lds_u128(unsigned addr) { return *(const LAS u32x4*)(uintptr_t)addr; }
DI void lds_w128(unsigned addr, u32x4 v) { *(LAS u32x4*)(uintptr_t)addr = v; }
DI s16x4 lds_tr(unsigned addr) { return __builtin_amdgcn_ds_read_tr16_b64_v4i16((LAS s16x4*)(uintptr_t)addr); }
DI bf16x8 cat(s16x4 a, s16x4 b) { return __builtin_shufflevector(a, b, 0, 1, 2, 3, 4, 5, 6, 7); }
DI void st_sc1(rsrc_t rs, unsigned off, u32x4 v) { __builtin_amdgcn_raw_buffer_store_b128(v, rs, off, 0, 16); }
DI u32x4 ld_sc1(rsrc_t rs, unsigned off) { return __builtin_amdgcn_raw_buffer_load_b128(rs, off, 0, 16); }
DI void st32_dev(void* p, unsigned v) { __hip_atomic_store((GAS unsigned*)p, v, __ATOMIC_RELAXED, __HIP_MEMORY_SCOPE_AGENT); }
DI unsigned ld32_dev(const void* p) { return __hip_atomic_load((GAS unsigned*)p, __ATOMIC_RELAXED, __HIP_MEMORY_SCOPE_AGENT); }
DI void wait_flags(const char* base, unsigned n, unsigned tag, unsigned lane) {
  const unsigned long long t0 = __builtin_amdgcn_s_memrealtime();
  for (;;) {
    const unsigned v = ld32_dev(base + 4 * (lane < n ? lane : 0));
    if (__all(v == tag)) break;
    if (__builtin_amdgcn_s_memrealtime() - t0 > 4000000ull) break;
    __builtin_amdgcn_s_sleep(1);
  }
  asm volatile("" ::: "memory");
}

__global__ __launch_bounds__(512, 2) void k_fused(Params p) {
  extern __shared__ __attribute__((aligned(16))) char smem[];
  const unsigned tid = threadIdx.x, lane = tid & 63, w = rfl(tid >> 6);
  const unsigned wg = blockIdx.x, kvh = wg & 7, j = wg >> 3, br = j >> 4, b = (j >> 1) & 7, sp = j & 1;
  const unsigned grp = br * 8 + b, mem = kvh * 2 + sp;
  const unsigned L0 = (unsigned)(uintptr_t)(LAS char*)smem;
  const unsigned g = w >> 2;
  STAMP(0);
  char* sync = p.ws + WS_SYNC + grp * 1024;
  unsigned tag = 0;
  if (w == 4) tag = ld32_dev(sync) + 1u;

  rsrc_t rx = mk_rsrc(p.x[br], NB * SQ * DM * 2), rwq = mk_rsrc(p.wq[br], DM * HQ * DH * 2), rwo = mk_rsrc(p.wo[br], HQ * DH * DM * 2);
  rsrc_t rk = mk_rsrc(p.k[br], NB * HKV * SK * DH * 2), rv = mk_rsrc(p.v[br], NB * HKV * SK * DH * 2);
  rsrc_t rws = mk_rsrc(p.ws, 64u << 20);

  const unsigned drow = 8 * (w >> 1) + ((lane >> 2) & 7), dch = 8 * (w & 1) + 4 * (lane >> 5) + ((lane & 3) ^ ((drow >> 2) & 3));
  const unsigned voKV = drow * 256 + dch * 16, voWQ = drow * 8192 + dch * 16, voWO = drow * 1024 + dch * 16;
  const unsigned kvbase = (b * HKV + kvh) * SK * 256;
  auto kvslot = [&](int u) -> unsigned { return (u & 3) * RSZ; };
  auto issue_k = [&](int u) {
    const unsigned slot = L0 + kvslot(u) + w * 1024, sk = kvbase + u * 16384;
#pragma unroll
    for (int i = 0; i < 2; ++i) dma16<KVNT>(rk, voKV, sk + 8192 * i, slot + 8192 * i);
  };
  auto issue_v = [&](int u) {
    const unsigned slot = L0 + kvslot(u) + w * 1024, sv = kvbase + (u - 1) * 16384;
#pragma unroll
    for (int i = 0; i < 2; ++i) dma16<KVNT>(rv, voKV, sv + 8192 * i, slot + 16384 + 8192 * i);
  };
  const unsigned wqcol = (kvh * 512 + sp * 256) * 2;
  auto wqslot = [&](int i) -> unsigned { return (1 + (i + 1) % 3) * RSZ; };
  auto issue_wq = [&](int i) {
    const unsigned kc = (i + b) & 7, slot = L0 + wqslot(i) + w * 1024;
#pragma unroll
    for (int hh = 0; hh < 2; ++hh)
#pragma unroll
      for (int ii = 0; ii < 2; ++ii) dma16<WQNT>(rwq, voWQ, (kc * 64 + 32 * ii) * 8192 + wqcol + hh * 256, slot + hh * 16384 + 8192 * ii);
  };
  const unsigned worow = kvh * 512 + sp * 256;
  auto issue_wo = [&](int i, unsigned slotoff) {
    const unsigned si = (i + b) & 7, kc = si >> 1, nh = si & 1, slot = L0 + slotoff + w * 1024;
#pragma unroll
    for (int tt = 0; tt < 2; ++tt)
#pragma unroll
      for (int ii = 0; ii < 2; ++ii) dma16<WONT>(rwo, voWO, (worow + kc * 64 + 32 * ii) * 1024 + (2 * nh + tt) * 256, slot + tt * 16384 + 8192 * ii);
  };

  const unsigned h = lane >> 5, r = lane & 31, blk = (lane >> 4) & 1, q4 = (lane & 15) >> 2, pp = lane & 3;
  const unsigned rr0 = 2048 * (r >> 3) + 64 * (r & 7) + 16 * ((0 + h) ^ ((r >> 2) & 3));
  const unsigned rr1 = 2048 * (r >> 3) + 64 * (r & 7) + 16 * ((2 + h) ^ ((r >> 2) & 3));
  const unsigned tp0 = 64 * (4 * h + q4) + 16 * ((2 * blk + (pp >> 1)) ^ (0 + h)) + 8 * (pp & 1);
  const unsigned tp1 = 2048 + 64 * (4 * h + q4) + 16 * ((2 * blk + (pp >> 1)) ^ (2 + h)) + 8 * (pp & 1);
  const unsigned hh = (w >> 1) & 1, kh = w & 1;

  bf16x8 qf[8];
  {
#pragma unroll
    for (int kt = 0; kt < 4; ++kt) dma16<WQNT>(rx, voWO, b * (SQ * DM * 2) + kt * 256, L0 + 4 * RSZ + kt * 8192 + w * 1024);
    issue_wq(0); issue_wq(1); issue_wq(2); issue_k(0);
    const unsigned ph = w >> 2, c = w & 3;
    const unsigned tn0 = 2048 * h + 64 * q4 + 16 * ((2 * blk + (pp >> 1)) ^ (2 * h)) + 8 * (pp & 1) + 512 * c + ph * 16384;
    const unsigned tn1 = 2048 * h + 256 + 64 * q4 + 16 * ((2 * blk + (pp >> 1)) ^ (2 * h + 1)) + 8 * (pp & 1) + 512 * c + ph * 16384;
    f32x16 acc = {};
#pragma unroll
    for (int i = 0; i < 8; ++i) {
      if (i == 0) WAIT_VM(10); else if (i < 3) WAIT_VM(6); else WAIT_VM(4);
      BARRIER();
      if (i == 0) STAMP(1);
      if (i >= 1 && i + 2 < 8) issue_wq(i + 2);
      if (i == 6) { issue_k(1); issue_v(1); }
      if (i == 7) { issue_k(2); issue_v(2); }
      const unsigned kc = (i + b) & 7, slot = L0 + wqslot(i);
      const unsigned xb = L0 + 4 * RSZ + (kc >> 1) * 8192 + (kc & 1) * 1024;
      s16x4 a0[4], a1[4]; bf16x8 xf[4];
#pragma unroll
      for (int ks = 0; ks < 4; ++ks) { a0[ks] = lds_tr(slot + tn0 + 4096 * ks); a1[ks] = lds_tr(slot + tn1 + 4096 * ks); xf[ks] = lds_b128(xb + ((ks & 1) ? rr1 : rr0) + 512 * (ks >> 1)); }
#pragma unroll
      for (int ks = 0; ks < 4; ++ks) acc = MFMA(cat(a0[ks], a1[ks]), xf[ks], acc);
    }
    BARRIER();
    STAMP(2);
    const unsigned QF = L0 + 4 * RSZ;
#pragma unroll
    for (int s = 0; s < 2; ++s) {
      unsigned a0 = cvtpk(acc[8 * s + 0] * SCL, acc[8 * s + 1] * SCL), a1 = cvtpk(acc[8 * s + 2] * SCL, acc[8 * s + 3] * SCL);
      unsigned b0 = cvtpk(acc[8 * s + 4] * SCL, acc[8 * s + 5] * SCL), b1 = cvtpk(acc[8 * s + 6] * SCL, acc[8 * s + 7] * SCL);
      auto r0 = __builtin_amdgcn_permlane32_swap(a0, b0, false, false); auto r1 = __builtin_amdgcn_permlane32_swap(a1, b1, false, false);
      u32x4 o = {r0[0], r1[0], r0[1], r1[1]};
      lds_w128(QF + (ph * 8 + 2 * c + s) * 1024 + lane * 16, o);
    }
    BARRIER();
#pragma unroll
    for (int s = 0; s < 8; ++s) qf[s] = lds_b128(QF + (hh * 8 + s) * 1024 + lane * 16);
    BARRIER();
    issue_wo(0, 4 * RSZ);
  }
  STAMP(3);

  const unsigned kb0 = L0 + 8192 * kh + rr0, kb1 = L0 + 8192 * kh + rr1, vb0 = L0 + 16384 + 8192 * kh + tp0, vb1 = L0 + 16384 + 8192 * kh + tp1;
  f32x16 o[4] = {};
  f32x16 negm = {};
  float m = 0.f, l = 0.f;
  bf16x8 pf[2] = {};
  constexpr unsigned DA = 2 * RSZ, DB = 3 * RSZ;
  const unsigned j4 = 2 * g + kh;
  f32x16 own;
  auto deposit = [&]() __attribute__((always_inline)) {
    l = swapadd(l);
    const float inv = 1.0f / l;
#pragma unroll
    for (int c = 0; c < 4; ++c) {
      if (j4 == (unsigned)c) own = o[c];
      else {
        const unsigned d = (j4 - c) & 3;
        const unsigned dst = L0 + (d == 1 ? DB + (hh * 4 + c) * 2048 : DA + ((hh * 4 + c) * 2 + (d - 2)) * 2048) + lane * 16;
#pragma unroll
        for (int s = 0; s < 2; ++s) {
          u32x4 u = {cvtpk(o[c][8 * s] * inv, o[c][8 * s + 1] * inv), cvtpk(o[c][8 * s + 2] * inv, o[c][8 * s + 3] * inv),
                     cvtpk(o[c][8 * s + 4] * inv, o[c][8 * s + 5] * inv), cvtpk(o[c][8 * s + 6] * inv, o[c][8 * s + 7] * inv)};
          lds_w128(dst + s * 1024, u);
        }
      }
    }
    *(LAS float*)(uintptr_t)(L0 + DB + 16384 + (hh * 4 + j4) * 256 + lane * 4) = h ? l : m;
  };
  s16x4 va[4][2] = {}, vc[4][2] = {};
  auto pv = [&]() __attribute__((always_inline)) {
#pragma unroll
    for (int ks = 0; ks < 2; ++ks)
#pragma unroll
      for (int c = 0; c < 4; ++c) o[c] = MFMA(cat(va[c][ks], vc[c][ks]), pf[ks], o[c]);
  };
  auto top = [&](int t, auto fast) __attribute__((always_inline)) {
    if constexpr (decltype(fast)::value) WAIT_VM(8);
    else { if (t <= NT - 3) WAIT_VM(8); else if (t < NT) WAIT_VM(6); else WAIT_VM(4); }
    BARRIER();
  };
  auto issue_next = [&](int t, auto fast) __attribute__((always_inline)) {
    if constexpr (decltype(fast)::value) {
      issue_k(t + 3); issue_v(t + 3);
    } else {
      if (t < NT - 3) { issue_k(t + 3); issue_v(t + 3); }
      else if (t == NT - 3) issue_v(NT);
      else if (t == NT - 2) issue_wo(1, 1 * RSZ);
    }
  };
  auto body = [&](int t, auto fast) __attribute__((always_inline)) {
    top(t, fast);
    if (t == 1) STAMP(5);
    bf16x8 kf[8];
    {
      const unsigned so = kvslot(t);
      const unsigned k0 = kb0 + so, k1 = kb1 + so;
#pragma unroll
      for (int k = 0; k < 8; ++k) kf[k] = lds_b128(((k & 1) ? k1 : k0) + 512 * (k >> 1));
      __builtin_amdgcn_sched_barrier(0);
      issue_next(t, fast);
      __builtin_amdgcn_sched_barrier(0);
      pv();
      __builtin_amdgcn_sched_barrier(0);
    }
    const int t1 = t + 1;
    top(t1, fast);
    if (t1 == 4) { STAMP(4); STAMPC(20); }
    if (t1 == 16) { STAMP(6); STAMPC(21); }
    {
      f32x16 s = MFMA(kf[0], qf[0], negm);
#pragma unroll
      for (int k = 1; k < 8; ++k) s = MFMA(kf[k], qf[k], s);
      __builtin_amdgcn_sched_barrier(0);
      const unsigned so = kvslot(t1);
      const unsigned v0 = vb0 + so, v1 = vb1 + so;
#pragma unroll
      for (int ks = 0; ks < 2; ++ks)
#pragma unroll
        for (int c = 0; c < 4; ++c) { va[c][ks] = lds_tr(v0 + 4096 * ks + 512 * c); vc[c][ks] = lds_tr(v1 + 4096 * ks + 512 * c); }
      issue_next(t1, fast);
      __builtin_amdgcn_sched_barrier(0);
      f32x16 e;
#pragma unroll
      for (int i = 0; i < 16; ++i) e[i] = __builtin_amdgcn_exp2f(s[i]);
      const float x0 = fmaxf(fmaxf(s[0], s[1]), s[2]), x1 = fmaxf(fmaxf(s[3], s[4]), s[5]), x2 = fmaxf(fmaxf(s[6], s[7]), s[8]), x3 = fmaxf(fmaxf(s[9], s[10]), s[11]), x4 = fmaxf(fmaxf(s[12], s[13]), s[14]);
      float mx = fmaxf(fmaxf(fmaxf(x0, x1), x2), fmaxf(fmaxf(x3, x4), s[15]));
      mx = swapmax(mx);
      if (__builtin_expect(t < 2 || __any(mx > THR), 0)) {
        const float dl = (t < 2) ? mx : fmaxf(mx, 0.f), al = (t < 2) ? 0.f : __builtin_amdgcn_exp2f(-dl);
        m += dl; l *= al;
#pragma unroll
        for (int i = 0; i < 16; ++i) e[i] = __builtin_amdgcn_exp2f(s[i] - dl);
        asm volatile("s_nop 1\n\tv_mfma_f32_32x32x2_f32 %0, %1, %2, 0\n\ts_nop 7\n\ts_nop 7\n\ts_nop 3" : "+v"(negm) : "v"(h ? 0.f : 1.f), "v"(-m));
#pragma unroll
        for (int c = 0; c < 4; ++c) o[c] *= al;
      }
#pragma unroll
      for (int ks = 0; ks < 2; ++ks) {
        u32x4 u = {cvtpk(e[8 * ks], e[8 * ks + 1]), cvtpk(e[8 * ks + 2], e[8 * ks + 3]), cvtpk(e[8 * ks + 4], e[8 * ks + 5]), cvtpk(e[8 * ks + 6], e[8 * ks + 7])};
        pf[ks] = __builtin_bit_cast(bf16x8, u);
      }
      l += ((e[0] + e[1]) + (e[2] + e[3])) + ((e[4] + e[5]) + (e[6] + e[7])) + (((e[8] + e[9]) + (e[10] + e[11])) + ((e[12] + e[13]) + (e[14] + e[15])));
    }
  };
  using FAST = std::integral_constant<bool, true>; using SLOW = std::integral_constant<bool, false>;
  if (g == 1) { top(0, FAST{}); issue_next(0, FAST{}); }
  int t = g;
  for (; t < 28; t += 2) body(t, FAST{});
  for (; t < NT; t += 2) body(t, SLOW{});
  if (g == 0) top(NT, SLOW{});
  pv();
  STAMP(7);
  deposit();
  BARRIER();
  issue_wo(2, 0 * RSZ);
  {
    u32x4 da[2], db[2], dc[2];
#pragma unroll
    for (int s = 0; s < 2; ++s) {
      da[s] = lds_u128(L0 + DB + (hh * 4 + j4) * 2048 + s * 1024 + lane * 16);
      db[s] = lds_u128(L0 + DA + ((hh * 4 + j4) * 2 + 0) * 2048 + s * 1024 + lane * 16);
      dc[s] = lds_u128(L0 + DA + ((hh * 4 + j4) * 2 + 1) * 2048 + s * 1024 + lane * 16);
    }
    const unsigned mlb = L0 + DB + 16384 + hh * 1024 + lane * 4;
    const float e1 = *(const LAS float*)(uintptr_t)(mlb + ((j4 + 1) & 3) * 256), e2 = *(const LAS float*)(uintptr_t)(mlb + ((j4 + 2) & 3) * 256), e3 = *(const LAS float*)(uintptr_t)(mlb + ((j4 + 3) & 3) * 256);
    const float m1 = lo_half(e1), l1 = hi_half(e1), m2 = lo_half(e2), l2 = hi_half(e2), m3 = lo_half(e3), l3 = hi_half(e3);
    const float M = fmaxf(fmaxf(m, m1), fmaxf(m2, m3));
    float a0 = __builtin_amdgcn_exp2f(m - M), w1 = l1 * __builtin_amdgcn_exp2f(m1 - M), w2 = l2 * __builtin_amdgcn_exp2f(m2 - M), w3 = l3 * __builtin_amdgcn_exp2f(m3 - M);
    const float inv = 1.0f / ((l * a0 + w1) + (w2 + w3));
    a0 *= inv; w1 *= inv; w2 *= inv; w3 *= inv;
#pragma unroll
    for (int s = 0; s < 2; ++s) {
      u32x4 u;
#pragma unroll
      for (int e = 0; e < 4; ++e) {
        const float lo = own[8 * s + 2 * e] * a0 + bflo(da[s][e]) * w1 + bflo(db[s][e]) * w2 + bflo(dc[s][e]) * w3;
        const float hi = own[8 * s + 2 * e + 1] * a0 + bfhi(da[s][e]) * w1 + bfhi(db[s][e]) * w2 + bfhi(dc[s][e]) * w3;
        u[e] = cvtpk(lo, hi);
      }
      lds_w128(L0 + DB + (hh * 4 + j4) * 2048 + s * 1024 + lane * 16, u);
    }
  }
  BARRIER();
  STAMP(10);
  issue_wo(3, DA);
  f32x16 acc[2] = {};
  {
    const unsigned wt0 = (w >> 2) * 16384 + 512 * (w & 3) + tp0, wt1 = (w >> 2) * 16384 + 512 * (w & 3) + tp1;
#pragma unroll
    for (int i = 0; i < 8; ++i) {
      if (i < 6) WAIT_VM(8); else if (i == 6) WAIT_VM(4); else WAIT_VM(0);
      if (i > 0) BARRIER();
      const unsigned so = ((i & 3) == 0 ? 4 : (i & 3) == 1 ? 1 : (i & 3) == 2 ? 0 : 2) * RSZ;
      if (i >= 1 && i + 3 < 8) issue_wo(i + 3, (((i + 3) & 3) == 0 ? 4 : ((i + 3) & 3) == 1 ? 1 : ((i + 3) & 3) == 2 ? 0 : 2) * RSZ);
      const unsigned kc = ((i + b) & 7) >> 1;
      const unsigned ofb = L0 + DB + kc * 4096 + lane * 16;
      s16x4 a0[4], a1[4]; bf16x8 of[4];
#pragma unroll
      for (int q = 0; q < 4; ++q) { a0[q] = lds_tr(L0 + so + wt0 + 4096 * q); a1[q] = lds_tr(L0 + so + wt1 + 4096 * q); of[q] = lds_b128(ofb + q * 1024); }
#pragma unroll
      for (int q = 0; q < 4; ++q) acc[i & 1] = MFMA(cat(a0[q], a1[q]), of[q], acc[i & 1]);
    }
  }
  STAMP(11);
#pragma unroll
  for (int nh = 0; nh < 2; ++nh)
#pragma unroll
    for (int q = 0; q < 4; ++q) {
      u32x4 v = {__float_as_uint(acc[nh][4 * q]), __float_as_uint(acc[nh][4 * q + 1]), __float_as_uint(acc[nh][4 * q + 2]), __float_as_uint(acc[nh][4 * q + 3])};
      st_sc1(rws, WS_YP + (grp * 16 + mem) * 65536 + ((((nh + b) & 1) * 8 + w) * 4 + q) * 1024 + lane * 16, v);
    }
  WAIT_VM(0);
  BARRIER();
  STAMP(12);
  if (w == 4) {
    if (lane == 0) st32_dev(sync + 128 + 4 * mem, tag);
    wait_flags(sync + 128, 16, tag, lane);
    if (lane == 0 && mem == 0) st32_dev(sync, tag);
    STAMP(13);
  }
  BARRIER();
  if (w < 4) {
    f32x4 a = {};
    u32x4 v[16];
#pragma unroll
    for (int i = 0; i < 16; ++i) v[i] = ld_sc1(rws, WS_YP + (grp * 16 + i) * 65536 + (mem * 4 + w) * 1024 + lane * 16);
#pragma unroll
    for (int i = 0; i < 16; ++i) { a[0] += __uint_as_float(v[i][0]); a[1] += __uint_as_float(v[i][1]); a[2] += __uint_as_float(v[i][2]); a[3] += __uint_as_float(v[i][3]); }
    const unsigned n = 32 * mem + 8 * w + 4 * h;
    u32x2 ov = {cvtpk(a[0], a[1]), cvtpk(a[2], a[3])};
    *(u32x2*)(p.out + ((size_t)(b * 32 + r) * 1024 + br * 512 + n) * 2) = ov;
  }
  STAMP(14);
}

extern "C" void kernel_launch(void* const* d_in, const int* in_sizes, int n_in, void* d_out, int out_size, void* d_ws, size_t ws_size, hipStream_t stream) {
  Params p;
  for (int i = 0; i < 2; ++i) {
    p.x[i] = (const char*)d_in[5 * i + 0]; p.k[i] = (const char*)d_in[5 * i + 1]; p.v[i] = (const char*)d_in[5 * i + 2];
    p.wq[i] = (const char*)d_in[5 * i + 3]; p.wo[i] = (const char*)d_in[5 * i + 4];
  }
  p.out = (char*)d_out; p.ws = (char*)d_ws;
  constexpr int SMEM = 5 * 32768;
  (void)hipFuncSetAttribute((const void*)k_fused, hipFuncAttributeMaxDynamicSharedMemorySize, SMEM);
  hipLaunchKernelGGL(k_fused, dim3(256), dim3(512), SMEM, stream, p);
}
```

```cpp
#include <hip/hip_runtime.h>
#include <stdint.h>
#include <type_traits>

typedef __attribute__((ext_vector_type(8))) short bf16x8;
typedef __attribute__((ext_vector_type(4))) short s16x4;
typedef __attribute__((ext_vector_type(16))) float f32x16;
typedef __attribute__((ext_vector_type(4))) float f32x4;
typedef __attribute__((ext_vector_type(2))) float f32x2;
typedef __attribute__((ext_vector_type(4))) unsigned u32x4;
typedef __attribute__((ext_vector_type(2))) unsigned u32x2;
typedef __attribute__((ext_vector_type(2))) __bf16 bf16x2;
#define LAS __attribute__((address_space(3)))
#define GAS __attribute__((address_space(1)))
#define DI __device__ __forceinline__
#define MFMA(a, b, c) __builtin_amdgcn_mfma_f32_32x32x16_bf16((a), (b), (c), 0, 0, 0)

constexpr int NB = 8, SQ = 32, DM = 512, HQ = 32, HKV = 8, SK = 2048, DH = 128;
constexpr float SCL = 0.088388347648318447f * 1.4426950408889634f;
constexpr float THR = 8.0f;
constexpr int NT = 32;
constexpr unsigned RSZ = 32768;

struct Params {
  const char* x[2]; const char* k[2]; const char* v[2]; const char* wq[2]; const char* wo[2];
  char* out; char* ws;
};
constexpr size_t WS_SYNC = 0;
constexpr size_t WS_YP = 1u << 20;
constexpr size_t WS_DBG = 128u << 20;

DI unsigned rfl(unsigned v) { return __builtin_amdgcn_readfirstlane(v); }
DI unsigned cvtpk(float lo, float hi) { f32x2 v = {lo, hi}; bf16x2 b = __builtin_convertvector(v, bf16x2); return __builtin_bit_cast(unsigned, b); }
DI float bflo(unsigned u) { return __uint_as_float(u << 16); }
DI float bfhi(unsigned u) { return __uint_as_float(u & 0xffff0000u); }
DI float swapmax(float x) { auto r = __builtin_amdgcn_permlane32_swap(__float_as_uint(x), __float_as_uint(x), false, false); return fmaxf(__uint_as_float(r[0]), __uint_as_float(r[1])); }
DI float swapadd(float x) { auto r = __builtin_amdgcn_permlane32_swap(__float_as_uint(x), __float_as_uint(x), false, false); return __uint_as_float(r[0]) + __uint_as_float(r[1]); }
DI float lo_half(float x) { auto r = __builtin_amdgcn_permlane32_swap(__float_as_uint(x), __float_as_uint(x), false, false); return __uint_as_float(r[0]); }
DI float hi_half(float x) { auto r = __builtin_amdgcn_permlane32_swap(__float_as_uint(x), __float_as_uint(x), false, false); return __uint_as_float(r[1]); }

typedef __amdgpu_buffer_rsrc_t rsrc_t;
DI rsrc_t mk_rsrc(const void* p, unsigned bytes) { return __builtin_amdgcn_make_buffer_rsrc((void*)p, 0, bytes, 0x00020000); }
template <bool NTP> DI void dma16(rsrc_t rs, unsigned voff, unsigned soff, unsigned ldsdst) {
  if constexpr (NTP) asm volatile("s_mov_b32 m0, %0\n\ts_nop 0\n\tbuffer_load_dwordx4 %1, %2, %3 offen nt lds" :: "s"(ldsdst), "v"(voff), "s"(rs), "s"(soff) : "memory");
  else               asm volatile("s_mov_b32 m0, %0\n\ts_nop 0\n\tbuffer_load_dwordx4 %1, %2, %3 offen lds" :: "s"(ldsdst), "v"(voff), "s"(rs), "s"(soff) : "memory");
}
#define KVNT false
#define WQNT false
#define YBF16 1
#define WONT false
#define STAMP(i) do {} while (0)
#define STAMPC(i) do {} while (0)
#define WAIT_VM(n) asm volatile("s_waitcnt vmcnt(%0)" :: "i"(n) : "memory")
#define BARRIER() asm volatile("s_waitcnt lgkmcnt(0)\n\ts_barrier" ::: "memory")

DI bf16x8 lds_b128(unsigned addr) { return *(const LAS bf16x8*)(uintptr_t)addr; }
DI u32x4 lds_u128(unsigned addr) { return *(const LAS u32x4*)(uintptr_t)addr; }
DI void lds_w128(unsigned addr, u32x4 v) { *(LAS u32x4*)(uintptr_t)addr = v; }
DI s16x4 lds_tr(unsigned addr) { return __builtin_amdgcn_ds_read_tr16_b64_v4i16((LAS s16x4*)(uintptr_t)addr); }
DI bf16x8 cat(s16x4 a, s16x4 b) { return __builtin_shufflevector(a, b, 0, 1, 2, 3, 4, 5, 6, 7); }
DI void st_sc1(rsrc_t rs, unsigned off, u32x4 v) { __builtin_amdgcn_raw_buffer_store_b128(v, rs, off, 0, 16); }
DI u32x4 ld_sc1(rsrc_t rs, unsigned off) { return __builtin_amdgcn_raw_buffer_load_b128(rs, off, 0, 16); }
DI void st32_dev(void* p, unsigned v) { __hip_atomic_store((GAS unsigned*)p, v, __ATOMIC_RELAXED, __HIP_MEMORY_SCOPE_AGENT); }
DI unsigned ld32_dev(const void* p) { return __hip_atomic_load((GAS unsigned*)p, __ATOMIC_RELAXED, __HIP_MEMORY_SCOPE_AGENT); }
DI void wait_flags(const char* base, unsigned n, unsigned tag, unsigned lane) {
  const unsigned long long t0 = __builtin_amdgcn_s_memrealtime();
  for (;;) {
    const unsigned v = ld32_dev(base + 4 * (lane < n ? lane : 0));
    if (__all(v == tag)) break;
    if (__builtin_amdgcn_s_memrealtime() - t0 > 4000000ull) break;
    __builtin_amdgcn_s_sleep(1);
  }
  asm volatile("" ::: "memory");
}

__global__ __launch_bounds__(512, 2) void k_fused(Params p) {
  extern __shared__ __attribute__((aligned(16))) char smem[];
  const unsigned tid = threadIdx.x, lane = tid & 63, w = rfl(tid >> 6);
  const unsigned wg = blockIdx.x, kvh = wg & 7, j = wg >> 3, br = j >> 4, b = (j >> 1) & 7, sp = j & 1;
  const unsigned grp = br * 8 + b, mem = kvh * 2 + sp;
  const unsigned L0 = (unsigned)(uintptr_t)(LAS char*)smem;
  const unsigned g = w >> 2;
  STAMP(0);
  char* sync = p.ws + WS_SYNC + grp * 1024;
  unsigned tag = 0;
  if (w == 4) tag = ld32_dev(sync) + 1u;

  rsrc_t rx = mk_rsrc(p.x[br], NB * SQ * DM * 2), rwq = mk_rsrc(p.wq[br], DM * HQ * DH * 2), rwo = mk_rsrc(p.wo[br], HQ * DH * DM * 2);
  rsrc_t rk = mk_rsrc(p.k[br], NB * HKV * SK * DH * 2), rv = mk_rsrc(p.v[br], NB * HKV * SK * DH * 2);
  rsrc_t rws = mk_rsrc(p.ws, 64u << 20);

  const unsigned drow = 8 * (w >> 1) + ((lane >> 2) & 7), dch = 8 * (w & 1) + 4 * (lane >> 5) + ((lane & 3) ^ ((drow >> 2) & 3));
  const unsigned voKV = drow * 256 + dch * 16, voWQ = drow * 8192 + dch * 16, voWO = drow * 1024 + dch * 16;
  const unsigned kvbase = (b * HKV + kvh) * SK * 256;
#define TSW 1
  const unsigned tsw = sp * TSW;
  auto kvslot = [&](int u) -> unsigned { return (u & 3) * RSZ; };
  auto issue_k = [&](int u) {
    const unsigned slot = L0 + kvslot(u) + w * 1024, sk = kvbase + (u ^ tsw) * 16384;
#pragma unroll
    for (int i = 0; i < 2; ++i) dma16<KVNT>(rk, voKV, sk + 8192 * i, slot + 8192 * i);
  };
  auto issue_v = [&](int u) {
    const unsigned slot = L0 + kvslot(u) + w * 1024, sv = kvbase + ((u - 1) ^ tsw) * 16384;
#pragma unroll
    for (int i = 0; i < 2; ++i) dma16<KVNT>(rv, voKV, sv + 8192 * i, slot + 16384 + 8192 * i);
  };
  const unsigned wqcol = (kvh * 512 + sp * 256) * 2;
  auto wqslot = [&](int i) -> unsigned { return (i & 3) * RSZ; };
  auto issue_wq = [&](int i) {
    const unsigned kc = (i + b) & 7, slot = L0 + wqslot(i) + w * 1024;
#pragma unroll
    for (int hh = 0; hh < 2; ++hh)
#pragma unroll
      for (int ii = 0; ii < 2; ++ii) dma16<WQNT>(rwq, voWQ, (kc * 64 + 32 * ii) * 8192 + wqcol + hh * 256, slot + hh * 16384 + 8192 * ii);
  };
  const unsigned worow = kvh * 512 + sp * 256;
  auto issue_wo = [&](int i, unsigned slotoff) {
    const unsigned kc = (i + b) & 3, nh = ((i >> 2) ^ (b >> 2)) & 1, slot = L0 + slotoff + w * 1024;
#pragma unroll
    for (int tt = 0; tt < 2; ++tt)
#pragma unroll
      for (int ii = 0; ii < 2; ++ii) dma16<WONT>(rwo, voWO, (worow + kc * 64 + 32 * ii) * 1024 + (2 * nh + tt) * 256, slot + tt * 16384 + 8192 * ii);
  };

  const unsigned h = lane >> 5, r = lane & 31, blk = (lane >> 4) & 1, q4 = (lane & 15) >> 2, pp = lane & 3;
  const unsigned rr0 = 2048 * (r >> 3) + 64 * (r & 7) + 16 * ((0 + h) ^ ((r >> 2) & 3));
  const unsigned rr1 = 2048 * (r >> 3) + 64 * (r & 7) + 16 * ((2 + h) ^ ((r >> 2) & 3));
  const unsigned tp0 = 64 * (4 * h + q4) + 16 * ((2 * blk + (pp >> 1)) ^ (0 + h)) + 8 * (pp & 1);
  const unsigned tp1 = 2048 + 64 * (4 * h + q4) + 16 * ((2 * blk + (pp >> 1)) ^ (2 + h)) + 8 * (pp & 1);
  const unsigned hh = (w >> 1) & 1, kh = w & 1;

  bf16x8 qf[8];
  {
#pragma unroll
    for (int kt = 0; kt < 4; ++kt) dma16<WQNT>(rx, voWO, b * (SQ * DM * 2) + kt * 256, L0 + 4 * RSZ + kt * 8192 + w * 1024);
    issue_wq(0); issue_wq(1); issue_wq(2); issue_wq(3);
    const unsigned ph = w >> 2, c = w & 3;
    const unsigned tn0 = 2048 * h + 64 * q4 + 16 * ((2 * blk + (pp >> 1)) ^ (2 * h)) + 8 * (pp & 1) + 512 * c + ph * 16384;
    const unsigned tn1 = 2048 * h + 256 + 64 * q4 + 16 * ((2 * blk + (pp >> 1)) ^ (2 * h + 1)) + 8 * (pp & 1) + 512 * c + ph * 16384;
    f32x16 acc = {};
#pragma unroll
    for (int i = 0; i < 8; ++i) {
      if (i == 0) WAIT_VM(12); else if (i <= 5) WAIT_VM(8); else if (i == 6) WAIT_VM(6); else WAIT_VM(6);
      BARRIER();
      if (i == 0) STAMP(1);
      if (i >= 1 && i + 3 < 8) issue_wq(i + 3);
      if (i == 5) issue_k(0);
      if (i == 6) { issue_k(1); issue_v(1); }
      if (i == 7) { issue_k(2); issue_v(2); }
      const unsigned kc = (i + b) & 7, slot = L0 + wqslot(i);
      const unsigned xb = L0 + 4 * RSZ + (kc >> 1) * 8192 + (kc & 1) * 1024;
      s16x4 a0[4], a1[4]; bf16x8 xf[4];
#pragma unroll
      for (int ks = 0; ks < 4; ++ks) { a0[ks] = lds_tr(slot + tn0 + 4096 * ks); a1[ks] = lds_tr(slot + tn1 + 4096 * ks); xf[ks] = lds_b128(xb + ((ks & 1) ? rr1 : rr0) + 512 * (ks >> 1)); }
#pragma unroll
      for (int ks = 0; ks < 4; ++ks) acc = MFMA(cat(a0[ks], a1[ks]), xf[ks], acc);
    }
    BARRIER();
    STAMP(2);
    const unsigned QF = L0 + 4 * RSZ;
#pragma unroll
    for (int s = 0; s < 2; ++s) {
      unsigned a0 = cvtpk(acc[8 * s + 0] * SCL, acc[8 * s + 1] * SCL), a1 = cvtpk(acc[8 * s + 2] * SCL, acc[8 * s + 3] * SCL);
      unsigned b0 = cvtpk(acc[8 * s + 4] * SCL, acc[8 * s + 5] * SCL), b1 = cvtpk(acc[8 * s + 6] * SCL, acc[8 * s + 7] * SCL);
      auto r0 = __builtin_amdgcn_permlane32_swap(a0, b0, false, false); auto r1 = __builtin_amdgcn_permlane32_swap(a1, b1, false, false);
      u32x4 o = {r0[0], r1[0], r0[1], r1[1]};
      lds_w128(QF + (ph * 8 + 2 * c + s) * 1024 + lane * 16, o);
    }
    BARRIER();
#pragma unroll
    for (int s = 0; s < 8; ++s) qf[s] = lds_b128(QF + (hh * 8 + s) * 1024 + lane * 16);
    BARRIER();
    issue_wo(0, 4 * RSZ);
  }
  STAMP(3);

  const unsigned kb0 = L0 + 8192 * kh + rr0, kb1 = L0 + 8192 * kh + rr1, vb0 = L0 + 16384 + 8192 * kh + tp0, vb1 = L0 + 16384 + 8192 * kh + tp1;
  f32x16 o[4] = {};
  f32x16 negm = {};
  float m = 0.f, l = 0.f;
  bf16x8 pf[2] = {};
  constexpr unsigned DA = 2 * RSZ, DB = 3 * RSZ;
  const unsigned j4 = 2 * g + kh;
  f32x16 own;
  auto deposit = [&]() __attribute__((always_inline)) {
    l = swapadd(l);
    const float inv = 1.0f / l;
#pragma unroll
    for (int c = 0; c < 4; ++c) {
      if (j4 == (unsigned)c) own = o[c];
      else {
        const unsigned d = (j4 - c) & 3;
        const unsigned dst = L0 + (d == 1 ? DB + (hh * 4 + c) * 2048 : DA + ((hh * 4 + c) * 2 + (d - 2)) * 2048) + lane * 16;
#pragma unroll
        for (int s = 0; s < 2; ++s) {
          u32x4 u = {cvtpk(o[c][8 * s] * inv, o[c][8 * s + 1] * inv), cvtpk(o[c][8 * s + 2] * inv, o[c][8 * s + 3] * inv),
                     cvtpk(o[c][8 * s + 4] * inv, o[c][8 * s + 5] * inv), cvtpk(o[c][8 * s + 6] * inv, o[c][8 * s + 7] * inv)};
          lds_w128(dst + s * 1024, u);
        }
      }
    }
    *(LAS float*)(uintptr_t)(L0 + DB + 16384 + (hh * 4 + j4) * 256 + lane * 4) = h ? l : m;
  };
  s16x4 va[4][2] = {}, vc[4][2] = {};
  auto pv = [&]() __attribute__((always_inline)) {
#pragma unroll
    for (int ks = 0; ks < 2; ++ks)
#pragma unroll
      for (int c = 0; c < 4; ++c) o[c] = MFMA(cat(va[c][ks], vc[c][ks]), pf[ks], o[c]);
  };
  auto top = [&](int t, auto fast) __attribute__((always_inline)) {
    if constexpr (decltype(fast)::value) WAIT_VM(8);
    else { if (t <= NT - 3) WAIT_VM(8); else if (t < NT) WAIT_VM(6); else WAIT_VM(4); }
    BARRIER();
  };
  auto issue_next = [&](int t, auto fast) __attribute__((always_inline)) {
    if constexpr (decltype(fast)::value) {
      issue_k(t + 3); issue_v(t + 3);
    } else {
      if (t < NT - 3) { issue_k(t + 3); issue_v(t + 3); }
      else if (t == NT - 3) issue_v(NT);
      else if (t == NT - 2) issue_wo(1, 1 * RSZ);
    }
  };
  auto body = [&](int t, auto fast) __attribute__((always_inline)) {
    top(t, fast);
    if (t == 1) STAMP(5);
    bf16x8 kf[8];
    {
      const unsigned so = kvslot(t);
      const unsigned k0 = kb0 + so, k1 = kb1 + so;
#pragma unroll
      for (int k = 0; k < 8; ++k) kf[k] = lds_b128(((k & 1) ? k1 : k0) + 512 * (k >> 1));
      __builtin_amdgcn_sched_barrier(0);
      issue_next(t, fast);
      __builtin_amdgcn_sched_barrier(0);
      pv();
      __builtin_amdgcn_sched_barrier(0);
    }
    const int t1 = t + 1;
    top(t1, fast);
    if (t1 == 4) { STAMP(4); STAMPC(20); }
    if (t1 == 16) { STAMP(6); STAMPC(21); }
    {
      f32x16 s = MFMA(kf[0], qf[0], negm);
#pragma unroll
      for (int k = 1; k < 8; ++k) s = MFMA(kf[k], qf[k], s);
      __builtin_amdgcn_sched_barrier(0);
      const unsigned so = kvslot(t1);
      const unsigned v0 = vb0 + so, v1 = vb1 + so;
#pragma unroll
      for (int ks = 0; ks < 2; ++ks)
#pragma unroll
        for (int c = 0; c < 4; ++c) { va[c][ks] = lds_tr(v0 + 4096 * ks + 512 * c); vc[c][ks] = lds_tr(v1 + 4096 * ks + 512 * c); }
      issue_next(t1, fast);
      __builtin_amdgcn_sched_barrier(0);
      f32x16 e;
#pragma unroll
      for (int i = 0; i < 16; ++i) e[i] = __builtin_amdgcn_exp2f(s[i]);
      const float x0 = fmaxf(fmaxf(s[0], s[1]), s[2]), x1 = fmaxf(fmaxf(s[3], s[4]), s[5]), x2 = fmaxf(fmaxf(s[6], s[7]), s[8]), x3 = fmaxf(fmaxf(s[9], s[10]), s[11]), x4 = fmaxf(fmaxf(s[12], s[13]), s[14]);
      float mx = fmaxf(fmaxf(fmaxf(x0, x1), x2), fmaxf(fmaxf(x3, x4), s[15]));
      mx = swapmax(mx);
      if (__builtin_expect(t < 2 || __any(mx > THR), 0)) {
        const float dl = (t < 2) ? mx : fmaxf(mx, 0.f), al = (t < 2) ? 0.f : __builtin_amdgcn_exp2f(-dl);
        m += dl; l *= al;
#pragma unroll
        for (int i = 0; i < 16; ++i) e[i] = __builtin_amdgcn_exp2f(s[i] - dl);
        asm volatile("s_nop 1\n\tv_mfma_f32_32x32x2_f32 %0, %1, %2, 0\n\ts_nop 7\n\ts_nop 7\n\ts_nop 3" : "+v"(negm) : "v"(h ? 0.f : 1.f), "v"(-m));
#pragma unroll
        for (int c = 0; c < 4; ++c) o[c] *= al;
      }
#pragma unroll
      for (int ks = 0; ks < 2; ++ks) {
        u32x4 u = {cvtpk(e[8 * ks], e[8 * ks + 1]), cvtpk(e[8 * ks + 2], e[8 * ks + 3]), cvtpk(e[8 * ks + 4], e[8 * ks + 5]), cvtpk(e[8 * ks + 6], e[8 * ks + 7])};
        pf[ks] = __builtin_bit_cast(bf16x8, u);
      }
      l += ((e[0] + e[1]) + (e[2] + e[3])) + ((e[4] + e[5]) + (e[6] + e[7])) + (((e[8] + e[9]) + (e[10] + e[11])) + ((e[12] + e[13]) + (e[14] + e[15])));
    }
  };
  using FAST = std::integral_constant<bool, true>; using SLOW = std::integral_constant<bool, false>;
  if (g == 1) { top(0, FAST{}); issue_next(0, FAST{}); }
  int t = g;
  for (; t < 28; t += 2) body(t, FAST{});
  for (; t < NT; t += 2) body(t, SLOW{});
  if (g == 0) top(NT, SLOW{});
  pv();
  STAMP(7);
  deposit();
  BARRIER();
  issue_wo(2, 0 * RSZ);
  {
    u32x4 da[2], db[2], dc[2];
#pragma unroll
    for (int s = 0; s < 2; ++s) {
      da[s] = lds_u128(L0 + DB + (hh * 4 + j4) * 2048 + s * 1024 + lane * 16);
      db[s] = lds_u128(L0 + DA + ((hh * 4 + j4) * 2 + 0) * 2048 + s * 1024 + lane * 16);
      dc[s] = lds_u128(L0 + DA + ((hh * 4 + j4) * 2 + 1) * 2048 + s * 1024 + lane * 16);
    }
    const unsigned mlb = L0 + DB + 16384 + hh * 1024 + lane * 4;
    const float e1 = *(const LAS float*)(uintptr_t)(mlb + ((j4 + 1) & 3) * 256), e2 = *(const LAS float*)(uintptr_t)(mlb + ((j4 + 2) & 3) * 256), e3 = *(const LAS float*)(uintptr_t)(mlb + ((j4 + 3) & 3) * 256);
    const float m1 = lo_half(e1), l1 = hi_half(e1), m2 = lo_half(e2), l2 = hi_half(e2), m3 = lo_half(e3), l3 = hi_half(e3);
    const float M = fmaxf(fmaxf(m, m1), fmaxf(m2, m3));
    float a0 = __builtin_amdgcn_exp2f(m - M), w1 = l1 * __builtin_amdgcn_exp2f(m1 - M), w2 = l2 * __builtin_amdgcn_exp2f(m2 - M), w3 = l3 * __builtin_amdgcn_exp2f(m3 - M);
    const float inv = 1.0f / ((l * a0 + w1) + (w2 + w3));
    a0 *= inv; w1 *= inv; w2 *= inv; w3 *= inv;
#pragma unroll
    for (int s = 0; s < 2; ++s) {
      u32x4 u;
#pragma unroll
      for (int e = 0; e < 4; ++e) {
        const float lo = own[8 * s + 2 * e] * a0 + bflo(da[s][e]) * w1 + bflo(db[s][e]) * w2 + bflo(dc[s][e]) * w3;
        const float hi = own[8 * s + 2 * e + 1] * a0 + bfhi(da[s][e]) * w1 + bfhi(db[s][e]) * w2 + bfhi(dc[s][e]) * w3;
        u[e] = cvtpk(lo, hi);
      }
      lds_w128(L0 + DB + (hh * 4 + j4) * 2048 + s * 1024 + lane * 16, u);
    }
  }
  BARRIER();
  STAMP(10);
  issue_wo(3, DA);
  f32x16 acc[2] = {};
  auto ystore = [&](int half) __attribute__((always_inline)) {
    const unsigned nh = (half ^ (b >> 2)) & 1;
#pragma unroll
    for (int q = 0; q < 2; ++q) {
      u32x4 v = {cvtpk(acc[half][8 * q], acc[half][8 * q + 1]), cvtpk(acc[half][8 * q + 2], acc[half][8 * q + 3]), cvtpk(acc[half][8 * q + 4], acc[half][8 * q + 5]), cvtpk(acc[half][8 * q + 6], acc[half][8 * q + 7])};
      st_sc1(rws, WS_YP + (grp * 16 + mem) * 32768 + ((nh * 8 + w) * 2 + q) * 1024 + lane * 16, v);
    }
  };
  constexpr int YS = 2;
  {
    const unsigned wt0 = (w >> 2) * 16384 + 512 * (w & 3) + tp0, wt1 = (w >> 2) * 16384 + 512 * (w & 3) + tp1;
#pragma unroll
    for (int i = 0; i < 8; ++i) {
      if (i < 5) WAIT_VM(8); else if (i == 5) WAIT_VM(8 + YS); else if (i == 6) WAIT_VM(4 + YS); else WAIT_VM(YS);
      if (i > 0) BARRIER();
      const unsigned so = ((i & 3) == 0 ? 4 : (i & 3) == 1 ? 1 : (i & 3) == 2 ? 0 : 2) * RSZ;
      if (i >= 1 && i + 3 < 8) issue_wo(i + 3, (((i + 3) & 3) == 0 ? 4 : ((i + 3) & 3) == 1 ? 1 : ((i + 3) & 3) == 2 ? 0 : 2) * RSZ);
      if (i == 4) ystore(0);
      const unsigned kc = (i + b) & 3;
      const unsigned ofb = L0 + DB + kc * 4096 + lane * 16;
      s16x4 a0[4], a1[4]; bf16x8 of[4];
#pragma unroll
      for (int q = 0; q < 4; ++q) { a0[q] = lds_tr(L0 + so + wt0 + 4096 * q); a1[q] = lds_tr(L0 + so + wt1 + 4096 * q); of[q] = lds_b128(ofb + q * 1024); }
#pragma unroll
      for (int q = 0; q < 4; ++q) acc[i >> 2] = MFMA(cat(a0[q], a1[q]), of[q], acc[i >> 2]);
    }
  }
  STAMP(11);
  ystore(1);
  WAIT_VM(0);
  BARRIER();
  STAMP(12);
  if (w == 4) {
    if (lane == 0) st32_dev(sync + 128 + 4 * mem, tag);
    wait_flags(sync + 128, 16, tag, lane);
    if (lane == 0 && mem == 0) st32_dev(sync, tag);
    STAMP(13);
  }
  BARRIER();
  if (w < 2) {
    u32x4 v[16];
#pragma unroll
    for (int i = 0; i < 16; ++i) v[i] = ld_sc1(rws, WS_YP + (grp * 16 + i) * 32768 + (mem * 2 + w) * 1024 + lane * 16);
    float a[8] = {};
#pragma unroll
    for (int i = 0; i < 16; ++i)
#pragma unroll
      for (int e = 0; e < 4; ++e) { a[2 * e] += bflo(v[i][e]); a[2 * e + 1] += bfhi(v[i][e]); }
    const unsigned n = 32 * mem + 16 * w + 4 * h;
    u32x2 o0 = {cvtpk(a[0], a[1]), cvtpk(a[2], a[3])}, o1 = {cvtpk(a[4], a[5]), cvtpk(a[6], a[7])};
    char* dst = p.out + ((size_t)(b * 32 + r) * 1024 + br * 512 + n) * 2;
    *(u32x2*)dst = o0; *(u32x2*)(dst + 16) = o1;
  }
  STAMP(14);
}

extern "C" void kernel_launch(void* const* d_in, const int* in_sizes, int n_in, void* d_out, int out_size, void* d_ws, size_t ws_size, hipStream_t stream) {
  Params p;
  for (int i = 0; i < 2; ++i) {
    p.x[i] = (const char*)d_in[5 * i + 0]; p.k[i] = (const char*)d_in[5 * i + 1]; p.v[i] = (const char*)d_in[5 * i + 2];
    p.wq[i] = (const char*)d_in[5 * i + 3]; p.wo[i] = (const char*)d_in[5 * i + 4];
  }
  p.out = (char*)d_out; p.ws = (char*)d_ws;
  constexpr int SMEM = 5 * 32768;
  (void)hipFuncSetAttribute((const void*)k_fused, hipFuncAttributeMaxDynamicSharedMemorySize, SMEM);
  hipLaunchKernelGGL(k_fused, dim3(256), dim3(512), SMEM, stream, p);
}
```

```cpp
#include <hip/hip_runtime.h>
#include <stdint.h>
#include <type_traits>

typedef __attribute__((ext_vector_type(8))) short bf16x8;
typedef __attribute__((ext_vector_type(4))) short s16x4;
typedef __attribute__((ext_vector_type(16))) float f32x16;
typedef __attribute__((ext_vector_type(4))) float f32x4;
typedef __attribute__((ext_vector_type(2))) float f32x2;
typedef __attribute__((ext_vector_type(4))) unsigned u32x4;
typedef __attribute__((ext_vector_type(2))) unsigned u32x2;
typedef __attribute__((ext_vector_type(2))) __bf16 bf16x2;
#define LAS __attribute__((address_space(3)))
#define GAS __attribute__((address_space(1)))
#define DI __device__ __forceinline__
#define MFMA(a, b, c) __builtin_amdgcn_mfma_f32_32x32x16_bf16((a), (b), (c), 0, 0, 0)

constexpr int NB = 8, SQ = 32, DM = 512, HQ = 32, HKV = 8, SK = 2048, DH = 128;
constexpr float SCL = 0.088388347648318447f * 1.4426950408889634f;
constexpr float THR = 8.0f;
constexpr int NT = 32;
constexpr unsigned RSZ = 32768;

struct Params {
  const char* x[2]; const char* k[2]; const char* v[2]; const char* wq[2]; const char* wo[2];
  char* out; char* ws;
};
constexpr size_t WS_SYNC = 0;
constexpr size_t WS_YP = 1u << 20;
constexpr size_t WS_DBG = 128u << 20;

DI unsigned rfl(unsigned v) { return __builtin_amdgcn_readfirstlane(v); }
DI unsigned cvtpk(float lo, float hi) { f32x2 v = {lo, hi}; bf16x2 b = __builtin_convertvector(v, bf16x2); return __builtin_bit_cast(unsigned, b); }
DI float bflo(unsigned u) { return __uint_as_float(u << 16); }
DI float bfhi(unsigned u) { return __uint_as_float(u & 0xffff0000u); }
DI float swapmax(float x) { auto r = __builtin_amdgcn_permlane32_swap(__float_as_uint(x), __float_as_uint(x), false, false); return fmaxf(__uint_as_float(r[0]), __uint_as_float(r[1])); }
DI float swapadd(float x) { auto r = __builtin_amdgcn_permlane32_swap(__float_as_uint(x), __float_as_uint(x), false, false); return __uint_as_float(r[0]) + __uint_as_float(r[1]); }
DI float lo_half(float x) { auto r = __builtin_amdgcn_permlane32_swap(__float_as_uint(x), __float_as_uint(x), false, false); return __uint_as_float(r[0]); }
DI float hi_half(float x) { auto r = __builtin_amdgcn_permlane32_swap(__float_as_uint(x), __float_as_uint(x), false, false); return __uint_as_float(r[1]); }

typedef __amdgpu_buffer_rsrc_t rsrc_t;
DI rsrc_t mk_rsrc(const void* p, unsigned bytes) { return __builtin_amdgcn_make_buffer_rsrc((void*)p, 0, bytes, 0x00020000); }
template <bool NTP> DI void dma16(rsrc_t rs, unsigned voff, unsigned soff, unsigned ldsdst) {
  if constexpr (NTP) asm volatile("s_mov_b32 m0, %0\n\ts_nop 0\n\tbuffer_load_dwordx4 %1, %2, %3 offen nt lds" :: "s"(ldsdst), "v"(voff), "s"(rs), "s"(soff) : "memory");
  else               asm volatile("s_mov_b32 m0, %0\n\ts_nop 0\n\tbuffer_load_dwordx4 %1, %2, %3 offen lds" :: "s"(ldsdst), "v"(voff), "s"(rs), "s"(soff) : "memory");
}
#define KVNT false
#define WQNT false
#define YBF16 1
#define WONT false
#define STAMP(i) do {} while (0)
#define STAMPC(i) do {} while (0)
#define WAIT_VM(n) asm volatile("s_waitcnt vmcnt(%0)" :: "i"(n) : "memory")
#define BARRIER() do { __builtin_amdgcn_sched_barrier(0); asm volatile("" ::: "memory"); __builtin_amdgcn_s_waitcnt(0xC07F); __builtin_amdgcn_s_barrier(); asm volatile("" ::: "memory"); __builtin_amdgcn_sched_barrier(0); } while (0)

DI bf16x8 lds_b128(unsigned addr) { return *(const LAS bf16x8*)(uintptr_t)addr; }
DI u32x4 lds_u128(unsigned addr) { return *(const LAS u32x4*)(uintptr_t)addr; }
DI void lds_w128(unsigned addr, u32x4 v) { *(LAS u32x4*)(uintptr_t)addr = v; }
DI s16x4 lds_tr(unsigned addr) { return __builtin_amdgcn_ds_read_tr16_b64_v4i16((LAS s16x4*)(uintptr_t)addr); }
DI bf16x8 cat(s16x4 a, s16x4 b) { return __builtin_shufflevector(a, b, 0, 1, 2, 3, 4, 5, 6, 7); }
DI void st_sc1(rsrc_t rs, unsigned off, u32x4 v) { __builtin_amdgcn_raw_buffer_store_b128(v, rs, off, 0, 16); }
DI u32x4 ld_sc1(rsrc_t rs, unsigned off) { return __builtin_amdgcn_raw_buffer_load_b128(rs, off, 0, 16); }
DI void st32_dev(void* p, unsigned v) { __hip_atomic_store((GAS unsigned*)p, v, __ATOMIC_RELAXED, __HIP_MEMORY_SCOPE_AGENT); }
DI unsigned ld32_dev(const void* p) { return __hip_atomic_load((GAS unsigned*)p, __ATOMIC_RELAXED, __HIP_MEMORY_SCOPE_AGENT); }
DI void wait_flags(const char* base, unsigned n, unsigned tag, unsigned lane) {
  const unsigned long long t0 = __builtin_amdgcn_s_memrealtime();
  for (;;) {
    const unsigned v = ld32_dev(base + 4 * (lane < n ? lane : 0));
    if (__all(v == tag)) break;
    if (__builtin_amdgcn_s_memrealtime() - t0 > 4000000ull) break;
    __builtin_amdgcn_s_sleep(1);
  }
  asm volatile("" ::: "memory");
}

__global__ __launch_bounds__(512, 2) void k_fused(Params p) {
  extern __shared__ __attribute__((aligned(16))) char smem[];
  const unsigned tid = threadIdx.x, lane = tid & 63, w = rfl(tid >> 6);
  const unsigned wg = blockIdx.x, kvh = wg & 7, j = wg >> 3, br = j >> 4, b = (j >> 1) & 7, sp = j & 1;
  const unsigned grp = br * 8 + b, mem = kvh * 2 + sp;
  const unsigned L0 = (unsigned)(uintptr_t)(LAS char*)smem;
  const unsigned g = w >> 2;
  STAMP(0);
  char* sync = p.ws + WS_SYNC + grp * 1024;
  unsigned tag = 0;
  if (w == 4) tag = ld32_dev(sync) + 1u;

  rsrc_t rx = mk_rsrc(p.x[br], NB * SQ * DM * 2), rwq = mk_rsrc(p.wq[br], DM * HQ * DH * 2), rwo = mk_rsrc(p.wo[br], HQ * DH * DM * 2);
  rsrc_t rk = mk_rsrc(p.k[br], NB * HKV * SK * DH * 2), rv = mk_rsrc(p.v[br], NB * HKV * SK * DH * 2);
  rsrc_t rws = mk_rsrc(p.ws, 64u << 20);

  const unsigned drow = 8 * (w >> 1) + ((lane >> 2) & 7), dch = 8 * (w & 1) + 4 * (lane >> 5) + ((lane & 3) ^ ((drow >> 2) & 3));
  const unsigned voKV = drow * 256 + dch * 16, voWQ = drow * 8192 + dch * 16, voWO = drow * 1024 + dch * 16;
  const unsigned kvbase = (b * HKV + kvh) * SK * 256;
#define TSW 1
  const unsigned tsw = sp * TSW;
  auto kvslot = [&](int u) -> unsigned { return (u & 3) * RSZ; };
  auto issue_k = [&](int u) {
    const unsigned slot = L0 + kvslot(u) + w * 1024, sk = kvbase + (u ^ tsw) * 16384;
#pragma unroll
    for (int i = 0; i < 2; ++i) dma16<KVNT>(rk, voKV, sk + 8192 * i, slot + 8192 * i);
  };
  auto issue_v = [&](int u) {
    const unsigned slot = L0 + kvslot(u) + w * 1024, sv = kvbase + ((u - 1) ^ tsw) * 16384;
#pragma unroll
    for (int i = 0; i < 2; ++i) dma16<KVNT>(rv, voKV, sv + 8192 * i, slot + 16384 + 8192 * i);
  };
  const unsigned wqcol = (kvh * 512 + sp * 256) * 2;
  auto wqslot = [&](int i) -> unsigned { return (i & 3) * RSZ; };
  auto issue_wq = [&](int i) {
    const unsigned kc = (i + b) & 7, slot = L0 + wqslot(i) + w * 1024;
#pragma unroll
    for (int hh = 0; hh < 2; ++hh)
#pragma unroll
      for (int ii = 0; ii < 2; ++ii) dma16<WQNT>(rwq, voWQ, (kc * 64 + 32 * ii) * 8192 + wqcol + hh * 256, slot + hh * 16384 + 8192 * ii);
  };
  const unsigned worow = kvh * 512 + sp * 256;
  auto issue_wo = [&](int i, unsigned slotoff) {
    const unsigned kc = (i + b) & 3, nh = ((i >> 2) ^ (b >> 2)) & 1, slot = L0 + slotoff + w * 1024;
#pragma unroll
    for (int tt = 0; tt < 2; ++tt)
#pragma unroll
      for (int ii = 0; ii < 2; ++ii) dma16<WONT>(rwo, voWO, (worow + kc * 64 + 32 * ii) * 1024 + (2 * nh + tt) * 256, slot + tt * 16384 + 8192 * ii);
  };

  const unsigned h = lane >> 5, r = lane & 31, blk = (lane >> 4) & 1, q4 = (lane & 15) >> 2, pp = lane & 3;
  const unsigned rr0 = 2048 * (r >> 3) + 64 * (r & 7) + 16 * ((0 + h) ^ ((r >> 2) & 3));
  const unsigned rr1 = 2048 * (r >> 3) + 64 * (r & 7) + 16 * ((2 + h) ^ ((r >> 2) & 3));
  const unsigned tp0 = 64 * (4 * h + q4) + 16 * ((2 * blk + (pp >> 1)) ^ (0 + h)) + 8 * (pp & 1);
  const unsigned tp1 = 2048 + 64 * (4 * h + q4) + 16 * ((2 * blk + (pp >> 1)) ^ (2 + h)) + 8 * (pp & 1);
  const unsigned hh = (w >> 1) & 1, kh = w & 1;

  bf16x8 qf[8];
  {
#pragma unroll
    for (int kt = 0; kt < 4; ++kt) dma16<WQNT>(rx, voWO, b * (SQ * DM * 2) + kt * 256, L0 + 4 * RSZ + kt * 8192 + w * 1024);
    issue_wq(0); issue_wq(1); issue_wq(2); issue_wq(3);
    const unsigned ph = w >> 2, c = w & 3;
    const unsigned tn0 = 2048 * h + 64 * q4 + 16 * ((2 * blk + (pp >> 1)) ^ (2 * h)) + 8 * (pp & 1) + 512 * c + ph * 16384;
    const unsigned tn1 = 2048 * h + 256 + 64 * q4 + 16 * ((2 * blk + (pp >> 1)) ^ (2 * h + 1)) + 8 * (pp & 1) + 512 * c + ph * 16384;
    f32x16 acc = {};
#pragma unroll
    for (int i = 0; i < 8; ++i) {
      if (i == 0) WAIT_VM(12); else if (i <= 5) WAIT_VM(8); else if (i == 6) WAIT_VM(6); else WAIT_VM(6);
      BARRIER();
      if (i == 0) STAMP(1);
      if (i >= 1 && i + 3 < 8) issue_wq(i + 3);
      if (i == 5) issue_k(0);
      if (i == 6) { issue_k(1); issue_v(1); }
      if (i == 7) { issue_k(2); issue_v(2); }
      const unsigned kc = (i + b) & 7, slot = L0 + wqslot(i);
      const unsigned xb = L0 + 4 * RSZ + (kc >> 1) * 8192 + (kc & 1) * 1024;
      s16x4 a0[4], a1[4]; bf16x8 xf[4];
#pragma unroll
      for (int ks = 0; ks < 4; ++ks) { a0[ks] = lds_tr(slot + tn0 + 4096 * ks); a1[ks] = lds_tr(slot + tn1 + 4096 * ks); xf[ks] = lds_b128(xb + ((ks & 1) ? rr1 : rr0) + 512 * (ks >> 1)); }
#pragma unroll
      for (int ks = 0; ks < 4; ++ks) acc = MFMA(cat(a0[ks], a1[ks]), xf[ks], acc);
    }
    BARRIER();
    STAMP(2);
    const unsigned QF = L0 + 4 * RSZ;
#pragma unroll
    for (int s = 0; s < 2; ++s) {
      unsigned a0 = cvtpk(acc[8 * s + 0] * SCL, acc[8 * s + 1] * SCL), a1 = cvtpk(acc[8 * s + 2] * SCL, acc[8 * s + 3] * SCL);
      unsigned b0 = cvtpk(acc[8 * s + 4] * SCL, acc[8 * s + 5] * SCL), b1 = cvtpk(acc[8 * s + 6] * SCL, acc[8 * s + 7] * SCL);
      auto r0 = __builtin_amdgcn_permlane32_swap(a0, b0, false, false); auto r1 = __builtin_amdgcn_permlane32_swap(a1, b1, false, false);
      u32x4 o = {r0[0], r1[0], r0[1], r1[1]};
      lds_w128(QF + (ph * 8 + 2 * c + s) * 1024 + lane * 16, o);
    }
    BARRIER();
#pragma unroll
    for (int s = 0; s < 8; ++s) qf[s] = lds_b128(QF + (hh * 8 + s) * 1024 + lane * 16);
    BARRIER();
    issue_wo(0, 4 * RSZ);
  }
  STAMP(3);

  const unsigned kb0 = L0 + 8192 * kh + rr0, kb1 = L0 + 8192 * kh + rr1, vb0 = L0 + 16384 + 8192 * kh + tp0, vb1 = L0 + 16384 + 8192 * kh + tp1;
  f32x16 o[4] = {};
  f32x16 negm = {};
  float m = 0.f, l = 0.f, l2 = 0.f;
  constexpr unsigned DA = 2 * RSZ, DB = 3 * RSZ;
  const unsigned j4 = 2 * g + kh;
  f32x16 own;
  auto deposit = [&]() __attribute__((always_inline)) {
    l = swapadd(l);
    const float inv = 1.0f / l;
#pragma unroll
    for (int c = 0; c < 4; ++c) {
      if (j4 == (unsigned)c) own = o[c];
      else {
        const unsigned d = (j4 - c) & 3;
        const unsigned dst = L0 + (d == 1 ? DB + (hh * 4 + c) * 2048 : DA + ((hh * 4 + c) * 2 + (d - 2)) * 2048) + lane * 16;
#pragma unroll
        for (int s = 0; s < 2; ++s) {
          u32x4 u = {cvtpk(o[c][8 * s] * inv, o[c][8 * s + 1] * inv), cvtpk(o[c][8 * s + 2] * inv, o[c][8 * s + 3] * inv),
                     cvtpk(o[c][8 * s + 4] * inv, o[c][8 * s + 5] * inv), cvtpk(o[c][8 * s + 6] * inv, o[c][8 * s + 7] * inv)};
          lds_w128(dst + s * 1024, u);
        }
      }
    }
    *(LAS float*)(uintptr_t)(L0 + DB + 16384 + (hh * 4 + j4) * 256 + lane * 4) = h ? l : m;
  };
  s16x4 va[4][2] = {}, vc[4][2] = {};
  f32x16 s;
#pragma unroll
  for (int i = 0; i < 16; ++i) s[i] = -__builtin_inff();
  auto finish = [&]() __attribute__((always_inline)) {
#pragma unroll
    for (int ks = 0; ks < 2; ++ks) {
      float e[8];
#pragma unroll
      for (int i = 0; i < 8; ++i) e[i] = __builtin_amdgcn_exp2f(s[8 * ks + i]);
      u32x4 u = {cvtpk(e[0], e[1]), cvtpk(e[2], e[3]), cvtpk(e[4], e[5]), cvtpk(e[6], e[7])};
      const bf16x8 pf = __builtin_bit_cast(bf16x8, u);
#pragma unroll
      for (int c = 0; c < 4; ++c) o[c] = MFMA(cat(va[c][ks], vc[c][ks]), pf, o[c]);
      if (ks == 0) l += ((e[0] + e[1]) + (e[2] + e[3])) + ((e[4] + e[5]) + (e[6] + e[7]));
      else l2 += ((e[0] + e[1]) + (e[2] + e[3])) + ((e[4] + e[5]) + (e[6] + e[7]));
    }
  };
  auto top = [&](int t, auto fast) __attribute__((always_inline)) {
    if constexpr (decltype(fast)::value) WAIT_VM(8);
    else { if (t <= NT - 3) WAIT_VM(8); else if (t < NT) WAIT_VM(6); else WAIT_VM(4); }
    BARRIER();
  };
  auto issue_next = [&](int t, auto fast) __attribute__((always_inline)) {
    if constexpr (decltype(fast)::value) {
      issue_k(t + 3); issue_v(t + 3);
    } else {
      if (t < NT - 3) { issue_k(t + 3); issue_v(t + 3); }
      else if (t == NT - 3) issue_v(NT);
      else if (t == NT - 2) issue_wo(1, 1 * RSZ);
    }
  };
  auto issue_piece = [&](int t, int q, auto fast) __attribute__((always_inline)) {
    if constexpr (decltype(fast)::value) {
      const int u = t + 3;
      const unsigned slot = L0 + kvslot(u) + w * 1024;
      if (q < 2) dma16<KVNT>(rk, voKV, kvbase + (u ^ tsw) * 16384 + 8192 * q, slot + 8192 * q);
      else dma16<KVNT>(rv, voKV, kvbase + ((u - 1) ^ tsw) * 16384 + 8192 * (q - 2), slot + 16384 + 8192 * (q - 2));
    } else { if (q == 0) issue_next(t, fast); }
  };
  auto body = [&](int t, auto fast) __attribute__((always_inline)) {
    top(t, fast);
    if (t == 1) STAMP(5);
    bf16x8 kf[8];
    {
      finish();
      __builtin_amdgcn_sched_barrier(0);
      const unsigned so = kvslot(t);
      const unsigned k0 = kb0 + so, k1 = kb1 + so;
#pragma unroll
      for (int k = 0; k < 8; ++k) kf[k] = lds_b128(((k & 1) ? k1 : k0) + 512 * (k >> 1));
      __builtin_amdgcn_sched_barrier(0);
      issue_next(t, fast);
      __builtin_amdgcn_sched_barrier(0);
    }
    const int t1 = t + 1;
    top(t1, fast);
    if (t1 == 4) { STAMP(4); STAMPC(20); }
    if (t1 == 16) { STAMP(6); STAMPC(21); }
    {
      const unsigned so = kvslot(t1);
      const unsigned v0 = vb0 + so, v1 = vb1 + so;
#pragma unroll
      for (int k = 0; k < 8; ++k) {
        s = MFMA(kf[k], qf[k], k == 0 ? negm : s);
        const int ks = k >> 2, c = k & 3;
        va[c][ks] = lds_tr(v0 + 4096 * ks + 512 * c); vc[c][ks] = lds_tr(v1 + 4096 * ks + 512 * c);
        if (k & 1) issue_piece(t1, k >> 1, fast);
        __builtin_amdgcn_sched_barrier(0);
      }
      const float x0 = fmaxf(fmaxf(s[0], s[1]), s[2]), x1 = fmaxf(fmaxf(s[3], s[4]), s[5]), x2 = fmaxf(fmaxf(s[6], s[7]), s[8]), x3 = fmaxf(fmaxf(s[9], s[10]), s[11]), x4 = fmaxf(fmaxf(s[12], s[13]), s[14]);
      float mx = fmaxf(fmaxf(fmaxf(x0, x1), x2), fmaxf(fmaxf(x3, x4), s[15]));
      mx = swapmax(mx);
      if (__builtin_expect(t < 2 || __any(mx > THR), 0)) {
        const float dl = (t < 2) ? mx : fmaxf(mx, 0.f), al = (t < 2) ? 0.f : __builtin_amdgcn_exp2f(-dl);
        m += dl; l *= al; l2 *= al;
#pragma unroll
        for (int i = 0; i < 16; ++i) s[i] -= dl;
        asm volatile("s_nop 1\n\tv_mfma_f32_32x32x2_f32 %0, %1, %2, 0\n\ts_nop 7\n\ts_nop 7\n\ts_nop 3" : "+v"(negm) : "v"(h ? 0.f : 1.f), "v"(-m));
#pragma unroll
        for (int c = 0; c < 4; ++c) o[c] *= al;
      }
    }
  };
  using FAST = std::integral_constant<bool, true>; using SLOW = std::integral_constant<bool, false>;
  if (g == 1) { top(0, FAST{}); issue_next(0, FAST{}); }
  int t = g;
  for (; t < 28; t += 2) body(t, FAST{});
  for (; t < NT; t += 2) body(t, SLOW{});
  if (g == 0) top(NT, SLOW{});
  finish();
  l += l2;
  STAMP(7);
  deposit();
  BARRIER();
  issue_wo(2, 0 * RSZ);
  {
    u32x4 da[2], db[2], dc[2];
#pragma unroll
    for (int s = 0; s < 2; ++s) {
      da[s] = lds_u128(L0 + DB + (hh * 4 + j4) * 2048 + s * 1024 + lane * 16);
      db[s] = lds_u128(L0 + DA + ((hh * 4 + j4) * 2 + 0) * 2048 + s * 1024 + lane * 16);
      dc[s] = lds_u128(L0 + DA + ((hh * 4 + j4) * 2 + 1) * 2048 + s * 1024 + lane * 16);
    }
    const unsigned mlb = L0 + DB + 16384 + hh * 1024 + lane * 4;
    const float e1 = *(const LAS float*)(uintptr_t)(mlb + ((j4 + 1) & 3) * 256), e2 = *(const LAS float*)(uintptr_t)(mlb + ((j4 + 2) & 3) * 256), e3 = *(const LAS float*)(uintptr_t)(mlb + ((j4 + 3) & 3) * 256);
    const float m1 = lo_half(e1), l1 = hi_half(e1), m2 = lo_half(e2), l2 = hi_half(e2), m3 = lo_half(e3), l3 = hi_half(e3);
    const float M = fmaxf(fmaxf(m, m1), fmaxf(m2, m3));
    float a0 = __builtin_amdgcn_exp2f(m - M), w1 = l1 * __builtin_amdgcn_exp2f(m1 - M), w2 = l2 * __builtin_amdgcn_exp2f(m2 - M), w3 = l3 * __builtin_amdgcn_exp2f(m3 - M);
    const float inv = 1.0f / ((l * a0 + w1) + (w2 + w3));
    a0 *= inv; w1 *= inv; w2 *= inv; w3 *= inv;
#pragma unroll
    for (int s = 0; s < 2; ++s) {
      u32x4 u;
#pragma unroll
      for (int e = 0; e < 4; ++e) {
        const float lo = own[8 * s + 2 * e] * a0 + bflo(da[s][e]) * w1 + bflo(db[s][e]) * w2 + bflo(dc[s][e]) * w3;
        const float hi = own[8 * s + 2 * e + 1] * a0 + bfhi(da[s][e]) * w1 + bfhi(db[s][e]) * w2 + bfhi(dc[s][e]) * w3;
        u[e] = cvtpk(lo, hi);
      }
      lds_w128(L0 + DB + (hh * 4 + j4) * 2048 + s * 1024 + lane * 16, u);
    }
  }
  BARRIER();
  STAMP(10);
  issue_wo(3, DA);
  f32x16 acc[2] = {};
  auto ystore = [&](int half) __attribute__((always_inline)) {
    const unsigned nh = (half ^ (b >> 2)) & 1;
#pragma unroll
    for (int q = 0; q < 2; ++q) {
      u32x4 v = {cvtpk(acc[half][8 * q], acc[half][8 * q + 1]), cvtpk(acc[half][8 * q + 2], acc[half][8 * q + 3]), cvtpk(acc[half][8 * q + 4], acc[half][8 * q + 5]), cvtpk(acc[half][8 * q + 6], acc[half][8 * q + 7])};
      st_sc1(rws, WS_YP + (grp * 16 + mem) * 32768 + ((nh * 8 + w) * 2 + q) * 1024 + lane * 16, v);
    }
  };
  constexpr int YS = 2;
  {
    const unsigned wt0 = (w >> 2) * 16384 + 512 * (w & 3) + tp0, wt1 = (w >> 2) * 16384 + 512 * (w & 3) + tp1;
#pragma unroll
    for (int i = 0; i < 8; ++i) {
      if (i < 5) WAIT_VM(8); else if (i == 5) WAIT_VM(8 + YS); else if (i == 6) WAIT_VM(4 + YS); else WAIT_VM(YS);
      if (i > 0) BARRIER();
      const unsigned so = ((i & 3) == 0 ? 4 : (i & 3) == 1 ? 1 : (i & 3) == 2 ? 0 : 2) * RSZ;
      if (i >= 1 && i + 3 < 8) issue_wo(i + 3, (((i + 3) & 3) == 0 ? 4 : ((i + 3) & 3) == 1 ? 1 : ((i + 3) & 3) == 2 ? 0 : 2) * RSZ);
      if (i == 4) ystore(0);
      const unsigned kc = (i + b) & 3;
      const unsigned ofb = L0 + DB + kc * 4096 + lane * 16;
      s16x4 a0[4], a1[4]; bf16x8 of[4];
#pragma unroll
      for (int q = 0; q < 4; ++q) { a0[q] = lds_tr(L0 + so + wt0 + 4096 * q); a1[q] = lds_tr(L0 + so + wt1 + 4096 * q); of[q] = lds_b128(ofb + q * 1024); }
#pragma unroll
      for (int q = 0; q < 4; ++q) acc[i >> 2] = MFMA(cat(a0[q], a1[q]), of[q], acc[i >> 2]);
    }
  }
  STAMP(11);
  ystore(1);
  WAIT_VM(0);
  BARRIER();
  STAMP(12);
  if (w == 4) {
    if (lane == 0) st32_dev(sync + 128 + 4 * mem, tag);
    wait_flags(sync + 128, 16, tag, lane);
    if (lane == 0 && mem == 0) st32_dev(sync, tag);
    STAMP(13);
  }
  BARRIER();
  if (w < 2) {
    u32x4 v[16];
#pragma unroll
    for (int i = 0; i < 16; ++i) v[i] = ld_sc1(rws, WS_YP + (grp * 16 + i) * 32768 + (mem * 2 + w) * 1024 + lane * 16);
    float a[8] = {};
#pragma unroll
    for (int i = 0; i < 16; ++i)
#pragma unroll
      for (int e = 0; e < 4; ++e) { a[2 * e] += bflo(v[i][e]); a[2 * e + 1] += bfhi(v[i][e]); }
    const unsigned n = 32 * mem + 16 * w + 4 * h;
    u32x2 o0 = {cvtpk(a[0], a[1]), cvtpk(a[2], a[3])}, o1 = {cvtpk(a[4], a[5]), cvtpk(a[6], a[7])};
    char* dst = p.out + ((size_t)(b * 32 + r) * 1024 + br * 512 + n) * 2;
    *(u32x2*)dst = o0; *(u32x2*)(dst + 16) = o1;
  }
  STAMP(14);
}

extern "C" void kernel_launch(void* const* d_in, const int* in_sizes, int n_in, void* d_out, int out_size, void* d_ws, size_t ws_size, hipStream_t stream) {
  Params p;
  for (int i = 0; i < 2; ++i) {
    p.x[i] = (const char*)d_in[5 * i + 0]; p.k[i] = (const char*)d_in[5 * i + 1]; p.v[i] = (const char*)d_in[5 * i + 2];
    p.wq[i] = (const char*)d_in[5 * i + 3]; p.wo[i] = (const char*)d_in[5 * i + 4];
  }
  p.out = (char*)d_out; p.ws = (char*)d_ws;
  constexpr int SMEM = 5 * 32768;
  (void)hipFuncSetAttribute((const void*)k_fused, hipFuncAttributeMaxDynamicSharedMemorySize, SMEM);
  hipLaunchKernelGGL(k_fused, dim3(256), dim3(512), SMEM, stream, p);
}
```

```cpp
#include <hip/hip_runtime.h>
#include <stdint.h>
#include <type_traits>

typedef __attribute__((ext_vector_type(8))) short bf16x8;
typedef __attribute__((ext_vector_type(4))) short s16x4;
typedef __attribute__((ext_vector_type(16))) float f32x16;
typedef __attribute__((ext_vector_type(4))) float f32x4;
typedef __attribute__((ext_vector_type(2))) float f32x2;
typedef __attribute__((ext_vector_type(4))) unsigned u32x4;
typedef __attribute__((ext_vector_type(2))) unsigned u32x2;
typedef __attribute__((ext_vector_type(2))) __bf16 bf16x2;
#define LAS __attribute__((address_space(3)))
#define GAS __attribute__((address_space(1)))
#define DI __device__ __forceinline__
#define MFMA(a, b, c) __builtin_amdgcn_mfma_f32_32x32x16_bf16((a), (b), (c), 0, 0, 0)

constexpr int NB = 8, SQ = 32, DM = 512, HQ = 32, HKV = 8, SK = 2048, DH = 128;
constexpr float SCL = 0.088388347648318447f * 1.4426950408889634f;
constexpr float THR = 8.0f;
constexpr int NT = 32;
constexpr unsigned RSZ = 32768;

struct Params {
  const char* x[2]; const char* k[2]; const char* v[2]; const char* wq[2]; const char* wo[2];
  char* out; char* ws;
};
constexpr size_t WS_SYNC = 0;
constexpr size_t WS_YP = 1u << 20;
constexpr size_t WS_DBG = 128u << 20;

DI unsigned rfl(unsigned v) { return __builtin_amdgcn_readfirstlane(v); }
DI unsigned cvtpk(float lo, float hi) { f32x2 v = {lo, hi}; bf16x2 b = __builtin_convertvector(v, bf16x2); return __builtin_bit_cast(unsigned, b); }
DI float bflo(unsigned u) { return __uint_as_float(u << 16); }
DI float bfhi(unsigned u) { return __uint_as_float(u & 0xffff0000u); }
DI float swapmax(float x) { auto r = __builtin_amdgcn_permlane32_swap(__float_as_uint(x), __float_as_uint(x), false, false); return fmaxf(__uint_as_float(r[0]), __uint_as_float(r[1])); }
DI float swapadd(float x) { auto r = __builtin_amdgcn_permlane32_swap(__float_as_uint(x), __float_as_uint(x), false, false); return __uint_as_float(r[0]) + __uint_as_float(r[1]); }
DI float lo_half(float x) { auto r = __builtin_amdgcn_permlane32_swap(__float_as_uint(x), __float_as_uint(x), false, false); return __uint_as_float(r[0]); }
DI float hi_half(float x) { auto r = __builtin_amdgcn_permlane32_swap(__float_as_uint(x), __float_as_uint(x), false, false); return __uint_as_float(r[1]); }

typedef __amdgpu_buffer_rsrc_t rsrc_t;
DI rsrc_t mk_rsrc(const void* p, unsigned bytes) { return __builtin_amdgcn_make_buffer_rsrc((void*)p, 0, bytes, 0x00020000); }
template <bool NTP> DI void dma16(rsrc_t rs, unsigned voff, unsigned soff, unsigned ldsdst) {
  if constexpr (NTP) asm volatile("s_mov_b32 m0, %0\n\ts_nop 0\n\tbuffer_load_dwordx4 %1, %2, %3 offen nt lds" :: "s"(ldsdst), "v"(voff), "s"(rs), "s"(soff) : "memory");
  else               asm volatile("s_mov_b32 m0, %0\n\ts_nop 0\n\tbuffer_load_dwordx4 %1, %2, %3 offen lds" :: "s"(ldsdst), "v"(voff), "s"(rs), "s"(soff) : "memory");
}
DI void dma16r(bool ntp, rsrc_t rs, unsigned voff, unsigned soff, unsigned ldsdst) { if (ntp) dma16<true>(rs, voff, soff, ldsdst); else dma16<false>(rs, voff, soff, ldsdst); }
#define ADT 10
#define ATH 100
#define KVNT false
#define WQNT false
#define YBF16 1
#define WONT false
#define STAMP(i) do {} while (0)
#define STAMPC(i) do {} while (0)
#define WAIT_VM(n) asm volatile("s_waitcnt vmcnt(%0)" :: "i"(n) : "memory")
#define BARRIER() do { __builtin_amdgcn_sched_barrier(0); asm volatile("" ::: "memory"); __builtin_amdgcn_s_waitcnt(0xC07F); __builtin_amdgcn_s_barrier(); asm volatile("" ::: "memory"); __builtin_amdgcn_sched_barrier(0); } while (0)

DI bf16x8 lds_b128(unsigned addr) { return *(const LAS bf16x8*)(uintptr_t)addr; }
DI u32x4 lds_u128(unsigned addr) { return *(const LAS u32x4*)(uintptr_t)addr; }
DI void lds_w128(unsigned addr, u32x4 v) { *(LAS u32x4*)(uintptr_t)addr = v; }
DI s16x4 lds_tr(unsigned addr) { return __builtin_amdgcn_ds_read_tr16_b64_v4i16((LAS s16x4*)(uintptr_t)addr); }
DI bf16x8 cat(s16x4 a, s16x4 b) { return __builtin_shufflevector(a, b, 0, 1, 2, 3, 4, 5, 6, 7); }
DI void st_sc1(rsrc_t rs, unsigned off, u32x4 v) { __builtin_amdgcn_raw_buffer_store_b128(v, rs, off, 0, 16); }
DI u32x4 ld_sc1(rsrc_t rs, unsigned off) { return __builtin_amdgcn_raw_buffer_load_b128(rs, off, 0, 16); }
DI void st32_dev(void* p, unsigned v) { __hip_atomic_store((GAS unsigned*)p, v, __ATOMIC_RELAXED, __HIP_MEMORY_SCOPE_AGENT); }
DI unsigned ld32_dev(const void* p) { return __hip_atomic_load((GAS unsigned*)p, __ATOMIC_RELAXED, __HIP_MEMORY_SCOPE_AGENT); }
DI void wait_flags(const char* base, unsigned n, unsigned tag, unsigned lane) {
  const unsigned long long t0 = __builtin_amdgcn_s_memrealtime();
  for (;;) {
    const unsigned v = ld32_dev(base + 4 * (lane < n ? lane : 0));
    if (__all(v == tag)) break;
    if (__builtin_amdgcn_s_memrealtime() - t0 > 4000000ull) break;
    __builtin_amdgcn_s_sleep(1);
  }
  asm volatile("" ::: "memory");
}

__global__ __launch_bounds__(512, 2) void k_fused(Params p) {
  extern __shared__ __attribute__((aligned(16))) char smem[];
  const unsigned tid = threadIdx.x, lane = tid & 63, w = rfl(tid >> 6);
  const unsigned wg = blockIdx.x, kvh = wg & 7, j = wg >> 3, br = j >> 4, b = (j >> 1) & 7, sp = j & 1;
  const unsigned grp = br * 8 + b, mem = kvh * 2 + sp;
  const unsigned L0 = (unsigned)(uintptr_t)(LAS char*)smem;
  const unsigned g = w >> 2;
  STAMP(0);
  char* sync = p.ws + WS_SYNC + grp * 1024;
  unsigned tag = 0;
  if (w == 4) tag = ld32_dev(sync) + 1u;

  rsrc_t rx = mk_rsrc(p.x[br], NB * SQ * DM * 2), rwq = mk_rsrc(p.wq[br], DM * HQ * DH * 2), rwo = mk_rsrc(p.wo[br], HQ * DH * DM * 2);
  rsrc_t rk = mk_rsrc(p.k[br], NB * HKV * SK * DH * 2), rv = mk_rsrc(p.v[br], NB * HKV * SK * DH * 2);
  rsrc_t rws = mk_rsrc(p.ws, 64u << 20);

  const unsigned drow = 8 * (w >> 1) + ((lane >> 2) & 7), dch = 8 * (w & 1) + 4 * (lane >> 5) + ((lane & 3) ^ ((drow >> 2) & 3));
  const unsigned voKV = drow * 256 + dch * 16, voWQ = drow * 8192 + dch * 16, voWO = drow * 1024 + dch * 16;
  const unsigned kvbase = (b * HKV + kvh) * SK * 256;
#define TSW 1
  const unsigned tsw = sp * TSW;
  auto kvslot = [&](int u) -> unsigned { return (u & 3) * RSZ; };
  bool usent = false;
  auto issue_k = [&](int u) __attribute__((always_inline)) {
    const unsigned slot = L0 + kvslot(u) + w * 1024, sk = kvbase + (u ^ tsw) * 16384;
    if (usent) { dma16<true>(rk, voKV, sk, slot); dma16<true>(rk, voKV, sk + 8192, slot + 8192); }
    else { dma16<false>(rk, voKV, sk, slot); dma16<false>(rk, voKV, sk + 8192, slot + 8192); }
  };
  auto issue_v = [&](int u) __attribute__((always_inline)) {
    const unsigned slot = L0 + kvslot(u) + w * 1024, sv = kvbase + ((u - 1) ^ tsw) * 16384;
    if (usent) { dma16<true>(rv, voKV, sv, slot + 16384); dma16<true>(rv, voKV, sv + 8192, slot + 16384 + 8192); }
    else { dma16<false>(rv, voKV, sv, slot + 16384); dma16<false>(rv, voKV, sv + 8192, slot + 16384 + 8192); }
  };
  const unsigned wqcol = (kvh * 512 + sp * 256) * 2;
  auto wqslot = [&](int i) -> unsigned { return (i & 3) * RSZ; };
  auto issue_wq = [&](int i) {
    const unsigned kc = (i + b) & 7, slot = L0 + wqslot(i) + w * 1024;
#pragma unroll
    for (int hh = 0; hh < 2; ++hh)
#pragma unroll
      for (int ii = 0; ii < 2; ++ii) dma16<WQNT>(rwq, voWQ, (kc * 64 + 32 * ii) * 8192 + wqcol + hh * 256, slot + hh * 16384 + 8192 * ii);
  };
  const unsigned worow = kvh * 512 + sp * 256;
  auto issue_wo = [&](int i, unsigned slotoff) {
    const unsigned kc = (i + b) & 3, nh = ((i >> 2) ^ (b >> 2)) & 1, slot = L0 + slotoff + w * 1024;
#pragma unroll
    for (int tt = 0; tt < 2; ++tt)
#pragma unroll
      for (int ii = 0; ii < 2; ++ii) dma16<WONT>(rwo, voWO, (worow + kc * 64 + 32 * ii) * 1024 + (2 * nh + tt) * 256, slot + tt * 16384 + 8192 * ii);
  };

  const unsigned h = lane >> 5, r = lane & 31, blk = (lane >> 4) & 1, q4 = (lane & 15) >> 2, pp = lane & 3;
  const unsigned rr0 = 2048 * (r >> 3) + 64 * (r & 7) + 16 * ((0 + h) ^ ((r >> 2) & 3));
  const unsigned rr1 = 2048 * (r >> 3) + 64 * (r & 7) + 16 * ((2 + h) ^ ((r >> 2) & 3));
  const unsigned tp0 = 64 * (4 * h + q4) + 16 * ((2 * blk + (pp >> 1)) ^ (0 + h)) + 8 * (pp & 1);
  const unsigned tp1 = 2048 + 64 * (4 * h + q4) + 16 * ((2 * blk + (pp >> 1)) ^ (2 + h)) + 8 * (pp & 1);
  const unsigned hh = (w >> 1) & 1, kh = w & 1;

  bf16x8 qf[8];
  {
#pragma unroll
    for (int kt = 0; kt < 4; ++kt) dma16<WQNT>(rx, voWO, b * (SQ * DM * 2) + kt * 256, L0 + 4 * RSZ + kt * 8192 + w * 1024);
    issue_wq(0); issue_wq(1); issue_wq(2); issue_wq(3);
    const unsigned ph = w >> 2, c = w & 3;
    const unsigned tn0 = 2048 * h + 64 * q4 + 16 * ((2 * blk + (pp >> 1)) ^ (2 * h)) + 8 * (pp & 1) + 512 * c + ph * 16384;
    const unsigned tn1 = 2048 * h + 256 + 64 * q4 + 16 * ((2 * blk + (pp >> 1)) ^ (2 * h + 1)) + 8 * (pp & 1) + 512 * c + ph * 16384;
    f32x16 acc = {};
#pragma unroll
    for (int i = 0; i < 8; ++i) {
      if (i == 0) WAIT_VM(12); else if (i <= 5) WAIT_VM(8); else if (i == 6) WAIT_VM(6); else WAIT_VM(6);
      BARRIER();
      if (i == 0) STAMP(1);
      if (i >= 1 && i + 3 < 8) issue_wq(i + 3);
      if (i == 5) issue_k(0);
      if (i == 6) { issue_k(1); issue_v(1); }
      if (i == 7) { issue_k(2); issue_v(2); }
      const unsigned kc = (i + b) & 7, slot = L0 + wqslot(i);
      const unsigned xb = L0 + 4 * RSZ + (kc >> 1) * 8192 + (kc & 1) * 1024;
      s16x4 a0[4], a1[4]; bf16x8 xf[4];
#pragma unroll
      for (int ks = 0; ks < 4; ++ks) { a0[ks] = lds_tr(slot + tn0 + 4096 * ks); a1[ks] = lds_tr(slot + tn1 + 4096 * ks); xf[ks] = lds_b128(xb + ((ks & 1) ? rr1 : rr0) + 512 * (ks >> 1)); }
#pragma unroll
      for (int ks = 0; ks < 4; ++ks) acc = MFMA(cat(a0[ks], a1[ks]), xf[ks], acc);
    }
    BARRIER();
    STAMP(2);
    const unsigned QF = L0 + 4 * RSZ;
#pragma unroll
    for (int s = 0; s < 2; ++s) {
      unsigned a0 = cvtpk(acc[8 * s + 0] * SCL, acc[8 * s + 1] * SCL), a1 = cvtpk(acc[8 * s + 2] * SCL, acc[8 * s + 3] * SCL);
      unsigned b0 = cvtpk(acc[8 * s + 4] * SCL, acc[8 * s + 5] * SCL), b1 = cvtpk(acc[8 * s + 6] * SCL, acc[8 * s + 7] * SCL);
      auto r0 = __builtin_amdgcn_permlane32_swap(a0, b0, false, false); auto r1 = __builtin_amdgcn_permlane32_swap(a1, b1, false, false);
      u32x4 o = {r0[0], r1[0], r0[1], r1[1]};
      lds_w128(QF + (ph * 8 + 2 * c + s) * 1024 + lane * 16, o);
    }
    BARRIER();
#pragma unroll
    for (int s = 0; s < 8; ++s) qf[s] = lds_b128(QF + (hh * 8 + s) * 1024 + lane * 16);
    BARRIER();
    issue_wo(0, 4 * RSZ);
  }
  STAMP(3);

  const unsigned kb0 = L0 + 8192 * kh + rr0, kb1 = L0 + 8192 * kh + rr1, vb0 = L0 + 16384 + 8192 * kh + tp0, vb1 = L0 + 16384 + 8192 * kh + tp1;
  f32x16 o[4] = {};
  f32x16 negm = {};
  float m = 0.f, l = 0.f, l2 = 0.f;
  constexpr unsigned DA = 2 * RSZ, DB = 3 * RSZ;
  const unsigned j4 = 2 * g + kh;
  f32x16 own;
  auto deposit = [&]() __attribute__((always_inline)) {
    l = swapadd(l);
    const float inv = 1.0f / l;
#pragma unroll
    for (int c = 0; c < 4; ++c) {
      if (j4 == (unsigned)c) own = o[c];
      else {
        const unsigned d = (j4 - c) & 3;
        const unsigned dst = L0 + (d == 1 ? DB + (hh * 4 + c) * 2048 : DA + ((hh * 4 + c) * 2 + (d - 2)) * 2048) + lane * 16;
#pragma unroll
        for (int s = 0; s < 2; ++s) {
          u32x4 u = {cvtpk(o[c][8 * s] * inv, o[c][8 * s + 1] * inv), cvtpk(o[c][8 * s + 2] * inv, o[c][8 * s + 3] * inv),
                     cvtpk(o[c][8 * s + 4] * inv, o[c][8 * s + 5] * inv), cvtpk(o[c][8 * s + 6] * inv, o[c][8 * s + 7] * inv)};
          lds_w128(dst + s * 1024, u);
        }
      }
    }
    *(LAS float*)(uintptr_t)(L0 + DB + 16384 + (hh * 4 + j4) * 256 + lane * 4) = h ? l : m;
  };
  s16x4 va[4][2] = {}, vc[4][2] = {};
  f32x16 s;
#pragma unroll
  for (int i = 0; i < 16; ++i) s[i] = -__builtin_inff();
  auto finish = [&]() __attribute__((always_inline)) {
#pragma unroll
    for (int ks = 0; ks < 2; ++ks) {
      float e[8];
#pragma unroll
      for (int i = 0; i < 8; ++i) e[i] = __builtin_amdgcn_exp2f(s[8 * ks + i]);
      u32x4 u = {cvtpk(e[0], e[1]), cvtpk(e[2], e[3]), cvtpk(e[4], e[5]), cvtpk(e[6], e[7])};
      const bf16x8 pf = __builtin_bit_cast(bf16x8, u);
#pragma unroll
      for (int c = 0; c < 4; ++c) o[c] = MFMA(cat(va[c][ks], vc[c][ks]), pf, o[c]);
      if (ks == 0) l += ((e[0] + e[1]) + (e[2] + e[3])) + ((e[4] + e[5]) + (e[6] + e[7]));
      else l2 += ((e[0] + e[1]) + (e[2] + e[3])) + ((e[4] + e[5]) + (e[6] + e[7]));
    }
  };
  auto top = [&](int t, auto fast) __attribute__((always_inline)) {
    if constexpr (decltype(fast)::value) WAIT_VM(8);
    else { if (t <= NT - 3) WAIT_VM(8); else if (t < NT) WAIT_VM(6); else WAIT_VM(4); }
    BARRIER();
  };
  auto issue_next = [&](int t, auto fast) __attribute__((always_inline)) {
    if constexpr (decltype(fast)::value) {
      issue_k(t + 3); issue_v(t + 3);
    } else {
      if (t < NT - 3) { issue_k(t + 3); issue_v(t + 3); }
      else if (t == NT - 3) issue_v(NT);
      else if (t == NT - 2) issue_wo(1, 1 * RSZ);
    }
  };
  auto issue_piece = [&](int t, int q, auto fast) __attribute__((always_inline)) {
    if constexpr (decltype(fast)::value) {
      if (q == 0) issue_k(t + 3);
      if (q == 2) issue_v(t + 3);
    } else { if (q == 0) issue_next(t, fast); }
  };
  unsigned long long tm0 = 0;
  auto body = [&](int t, auto fast) __attribute__((always_inline)) {
    top(t, fast);
    if constexpr (decltype(fast)::value) {
      if (t - (int)g == 4) tm0 = __builtin_amdgcn_s_memrealtime();
      if (t - (int)g == ADT) usent = (__builtin_amdgcn_s_memrealtime() - tm0) > (unsigned long long)((ADT - 4) * ATH);
    }
    __builtin_amdgcn_s_setprio(0);
    if (t == 1) STAMP(5);
    bf16x8 kf[8];
    {
      finish();
      __builtin_amdgcn_sched_barrier(0);
      const unsigned so = kvslot(t);
      const unsigned k0 = kb0 + so, k1 = kb1 + so;
#pragma unroll
      for (int k = 0; k < 8; ++k) kf[k] = lds_b128(((k & 1) ? k1 : k0) + 512 * (k >> 1));
      __builtin_amdgcn_sched_barrier(0);
      issue_next(t, fast);
      __builtin_amdgcn_sched_barrier(0);
    }
    const int t1 = t + 1;
    top(t1, fast);
    __builtin_amdgcn_s_setprio(2);
    if (t1 == 4) { STAMP(4); STAMPC(20); }
    if (t1 == 16) { STAMP(6); STAMPC(21); }
    {
      const unsigned so = kvslot(t1);
      const unsigned v0 = vb0 + so, v1 = vb1 + so;
#pragma unroll
      for (int k = 0; k < 8; ++k) {
        s = MFMA(kf[k], qf[k], k == 0 ? negm : s);
        const int ks = k >> 2, c = k & 3;
        va[c][ks] = lds_tr(v0 + 4096 * ks + 512 * c); vc[c][ks] = lds_tr(v1 + 4096 * ks + 512 * c);
        if (k & 1) issue_piece(t1, k >> 1, fast);
        __builtin_amdgcn_sched_barrier(0);
      }
      const float x0 = fmaxf(fmaxf(s[0], s[1]), s[2]), x1 = fmaxf(fmaxf(s[3], s[4]), s[5]), x2 = fmaxf(fmaxf(s[6], s[7]), s[8]), x3 = fmaxf(fmaxf(s[9], s[10]), s[11]), x4 = fmaxf(fmaxf(s[12], s[13]), s[14]);
      float mx = fmaxf(fmaxf(fmaxf(x0, x1), x2), fmaxf(fmaxf(x3, x4), s[15]));
      mx = swapmax(mx);
      if (__builtin_expect(t < 2 || __any(mx > THR), 0)) {
        const float dl = (t < 2) ? mx : fmaxf(mx, 0.f), al = (t < 2) ? 0.f : __builtin_amdgcn_exp2f(-dl);
        m += dl; l *= al; l2 *= al;
#pragma unroll
        for (int i = 0; i < 16; ++i) s[i] -= dl;
        asm volatile("s_nop 1\n\tv_mfma_f32_32x32x2_f32 %0, %1, %2, 0\n\ts_nop 7\n\ts_nop 7\n\ts_nop 3" : "+v"(negm) : "v"(h ? 0.f : 1.f), "v"(-m));
#pragma unroll
        for (int c = 0; c < 4; ++c) o[c] *= al;
      }
    }
  };
  using FAST = std::integral_constant<bool, true>; using SLOW = std::integral_constant<bool, false>;
  if (g == 1) { top(0, FAST{}); issue_next(0, FAST{}); }
  int t = g;
  for (; t < 28; t += 2) body(t, FAST{});
  for (; t < NT; t += 2) body(t, SLOW{});
  if (g == 0) top(NT, SLOW{});
  __builtin_amdgcn_s_setprio(0);
  finish();
  l += l2;
  STAMP(7);
  deposit();
  BARRIER();
  issue_wo(2, 0 * RSZ);
  {
    u32x4 da[2], db[2], dc[2];
#pragma unroll
    for (int s = 0; s < 2; ++s) {
      da[s] = lds_u128(L0 + DB + (hh * 4 + j4) * 2048 + s * 1024 + lane * 16);
      db[s] = lds_u128(L0 + DA + ((hh * 4 + j4) * 2 + 0) * 2048 + s * 1024 + lane * 16);
      dc[s] = lds_u128(L0 + DA + ((hh * 4 + j4) * 2 + 1) * 2048 + s * 1024 + lane * 16);
    }
    const unsigned mlb = L0 + DB + 16384 + hh * 1024 + lane * 4;
    const float e1 = *(const LAS float*)(uintptr_t)(mlb + ((j4 + 1) & 3) * 256), e2 = *(const LAS float*)(uintptr_t)(mlb + ((j4 + 2) & 3) * 256), e3 = *(const LAS float*)(uintptr_t)(mlb + ((j4 + 3) & 3) * 256);
    const float m1 = lo_half(e1), l1 = hi_half(e1), m2 = lo_half(e2), l2 = hi_half(e2), m3 = lo_half(e3), l3 = hi_half(e3);
    const float M = fmaxf(fmaxf(m, m1), fmaxf(m2, m3));
    float a0 = __builtin_amdgcn_exp2f(m - M), w1 = l1 * __builtin_amdgcn_exp2f(m1 - M), w2 = l2 * __builtin_amdgcn_exp2f(m2 - M), w3 = l3 * __builtin_amdgcn_exp2f(m3 - M);
    const float inv = 1.0f / ((l * a0 + w1) + (w2 + w3));
    a0 *= inv; w1 *= inv; w2 *= inv; w3 *= inv;
#pragma unroll
    for (int s = 0; s < 2; ++s) {
      u32x4 u;
#pragma unroll
      for (int e = 0; e < 4; ++e) {
        const float lo = own[8 * s + 2 * e] * a0 + bflo(da[s][e]) * w1 + bflo(db[s][e]) * w2 + bflo(dc[s][e]) * w3;
        const float hi = own[8 * s + 2 * e + 1] * a0 + bfhi(da[s][e]) * w1 + bfhi(db[s][e]) * w2 + bfhi(dc[s][e]) * w3;
        u[e] = cvtpk(lo, hi);
      }
      lds_w128(L0 + DB + (hh * 4 + j4) * 2048 + s * 1024 + lane * 16, u);
    }
  }
  BARRIER();
  STAMP(10);
  issue_wo(3, DA);
  f32x16 acc[2] = {};
  auto ystore = [&](int half) __attribute__((always_inline)) {
    const unsigned nh = (half ^ (b >> 2)) & 1;
#pragma unroll
    for (int q = 0; q < 2; ++q) {
      u32x4 v = {cvtpk(acc[half][8 * q], acc[half][8 * q + 1]), cvtpk(acc[half][8 * q + 2], acc[half][8 * q + 3]), cvtpk(acc[half][8 * q + 4], acc[half][8 * q + 5]), cvtpk(acc[half][8 * q + 6], acc[half][8 * q + 7])};
      st_sc1(rws, WS_YP + (grp * 16 + mem) * 32768 + ((nh * 8 + w) * 2 + q) * 1024 + lane * 16, v);
    }
  };
  constexpr int YS = 2;
  {
    const unsigned wt0 = (w >> 2) * 16384 + 512 * (w & 3) + tp0, wt1 = (w >> 2) * 16384 + 512 * (w & 3) + tp1;
#pragma unroll
    for (int i = 0; i < 8; ++i) {
      if (i < 5) WAIT_VM(8); else if (i == 5) WAIT_VM(8 + YS); else if (i == 6) WAIT_VM(4 + YS); else WAIT_VM(YS);
      if (i > 0) BARRIER();
      const unsigned so = ((i & 3) == 0 ? 4 : (i & 3) == 1 ? 1 : (i & 3) == 2 ? 0 : 2) * RSZ;
      if (i >= 1 && i + 3 < 8) issue_wo(i + 3, (((i + 3) & 3) == 0 ? 4 : ((i + 3) & 3) == 1 ? 1 : ((i + 3) & 3) == 2 ? 0 : 2) * RSZ);
      if (i == 4) ystore(0);
      const unsigned kc = (i + b) & 3;
      const unsigned ofb = L0 + DB + kc * 4096 + lane * 16;
      s16x4 a0[4], a1[4]; bf16x8 of[4];
#pragma unroll
      for (int q = 0; q < 4; ++q) { a0[q] = lds_tr(L0 + so + wt0 + 4096 * q); a1[q] = lds_tr(L0 + so + wt1 + 4096 * q); of[q] = lds_b128(ofb + q * 1024); }
#pragma unroll
      for (int q = 0; q < 4; ++q) acc[i >> 2] = MFMA(cat(a0[q], a1[q]), of[q], acc[i >> 2]);
    }
  }
  STAMP(11);
  ystore(1);
  WAIT_VM(0);
  BARRIER();
  STAMP(12);
  if (w == 4) {
    if (lane == 0) st32_dev(sync + 128 + 4 * mem, tag);
    wait_flags(sync + 128, 16, tag, lane);
    if (lane == 0 && mem == 0) st32_dev(sync, tag);
    STAMP(13);
  }
  BARRIER();
  if (w < 2) {
    u32x4 v[16];
#pragma unroll
    for (int i = 0; i < 16; ++i) v[i] = ld_sc1(rws, WS_YP + (grp * 16 + i) * 32768 + (mem * 2 + w) * 1024 + lane * 16);
    float a[8] = {};
#pragma unroll
    for (int i = 0; i < 16; ++i)
#pragma unroll
      for (int e = 0; e < 4; ++e) { a[2 * e] += bflo(v[i][e]); a[2 * e + 1] += bfhi(v[i][e]); }
    const unsigned n = 32 * mem + 16 * w + 4 * h;
    u32x2 o0 = {cvtpk(a[0], a[1]), cvtpk(a[2], a[3])}, o1 = {cvtpk(a[4], a[5]), cvtpk(a[6], a[7])};
    char* dst = p.out + ((size_t)(b * 32 + r) * 1024 + br * 512 + n) * 2;
    *(u32x2*)dst = o0; *(u32x2*)(dst + 16) = o1;
  }
  STAMP(14);
}

extern "C" void kernel_launch(void* const* d_in, const int* in_sizes, int n_in, void* d_out, int out_size, void* d_ws, size_t ws_size, hipStream_t stream) {
  Params p;
  for (int i = 0; i < 2; ++i) {
    p.x[i] = (const char*)d_in[5 * i + 0]; p.k[i] = (const char*)d_in[5 * i + 1]; p.v[i] = (const char*)d_in[5 * i + 2];
    p.wq[i] = (const char*)d_in[5 * i + 3]; p.wo[i] = (const char*)d_in[5 * i + 4];
  }
  p.out = (char*)d_out; p.ws = (char*)d_ws;
  constexpr int SMEM = 5 * 32768;
  (void)hipFuncSetAttribute((const void*)k_fused, hipFuncAttributeMaxDynamicSharedMemorySize, SMEM);
  hipLaunchKernelGGL(k_fused, dim3(256), dim3(512), SMEM, stream, p);
}
```

```cpp
#include <hip/hip_runtime.h>
#include <stdint.h>
#include <type_traits>

typedef __attribute__((ext_vector_type(8))) short bf16x8;
typedef __attribute__((ext_vector_type(4))) short s16x4;
typedef __attribute__((ext_vector_type(16))) float f32x16;
typedef __attribute__((ext_vector_type(4))) float f32x4;
typedef __attribute__((ext_vector_type(2))) float f32x2;
typedef __attribute__((ext_vector_type(4))) unsigned u32x4;
typedef __attribute__((ext_vector_type(2))) unsigned u32x2;
typedef __attribute__((ext_vector_type(2))) __bf16 bf16x2;
#define LAS __attribute__((address_space(3)))
#define GAS __attribute__((address_space(1)))
#define DI __device__ __forceinline__
#define MFMA(a, b, c) __builtin_amdgcn_mfma_f32_32x32x16_bf16((a), (b), (c), 0, 0, 0)

constexpr int NB = 8, SQ = 32, DM = 512, HQ = 32, HKV = 8, SK = 2048, DH = 128;
constexpr float SCL = 0.088388347648318447f * 1.4426950408889634f;
constexpr float THR = 8.0f;
constexpr int NT = 32;
constexpr unsigned RSZ = 32768;

struct Params {
  const char* x[2]; const char* k[2]; const char* v[2]; const char* wq[2]; const char* wo[2];
  char* out; char* ws;
};
constexpr size_t WS_SYNC = 0;
constexpr size_t WS_YP = 1u << 20;
constexpr size_t WS_DBG = 128u << 20;

DI unsigned rfl(unsigned v) { return __builtin_amdgcn_readfirstlane(v); }
DI unsigned cvtpk(float lo, float hi) { f32x2 v = {lo, hi}; bf16x2 b = __builtin_convertvector(v, bf16x2); return __builtin_bit_cast(unsigned, b); }
DI float bflo(unsigned u) { return __uint_as_float(u << 16); }
DI float bfhi(unsigned u) { return __uint_as_float(u & 0xffff0000u); }
DI float swapmax(float x) { auto r = __builtin_amdgcn_permlane32_swap(__float_as_uint(x), __float_as_uint(x), false, false); return fmaxf(__uint_as_float(r[0]), __uint_as_float(r[1])); }
DI float swapadd(float x) { auto r = __builtin_amdgcn_permlane32_swap(__float_as_uint(x), __float_as_uint(x), false, false); return __uint_as_float(r[0]) + __uint_as_float(r[1]); }
DI float lo_half(float x) { auto r = __builtin_amdgcn_permlane32_swap(__float_as_uint(x), __float_as_uint(x), false, false); return __uint_as_float(r[0]); }
DI float hi_half(float x) { auto r = __builtin_amdgcn_permlane32_swap(__float_as_uint(x), __float_as_uint(x), false, false); return __uint_as_float(r[1]); }

typedef __amdgpu_buffer_rsrc_t rsrc_t;
DI rsrc_t mk_rsrc(const void* p, unsigned bytes) { return __builtin_amdgcn_make_buffer_rsrc((void*)p, 0, bytes, 0x00020000); }
template <bool NTP> DI void dma16(rsrc_t rs, unsigned voff, unsigned soff, unsigned ldsdst) {
  if constexpr (NTP) asm volatile("s_mov_b32 m0, %0\n\ts_nop 0\n\tbuffer_load_dwordx4 %1, %2, %3 offen nt lds" :: "s"(ldsdst), "v"(voff), "s"(rs), "s"(soff) : "memory");
  else               asm volatile("s_mov_b32 m0, %0\n\ts_nop 0\n\tbuffer_load_dwordx4 %1, %2, %3 offen lds" :: "s"(ldsdst), "v"(voff), "s"(rs), "s"(soff) : "memory");
}
#define KVNT false
#define WQNT false
#define YBF16 1
#define WONT false
#define STAMP(i) do {} while (0)
#define STAMPC(i) do {} while (0)
#define WAIT_VM(n) asm volatile("s_waitcnt vmcnt(%0)" :: "i"(n) : "memory")
#define BARRIER() do { __builtin_amdgcn_sched_barrier(0); asm volatile("" ::: "memory"); __builtin_amdgcn_s_waitcnt(0xC07F); __builtin_amdgcn_s_barrier(); asm volatile("" ::: "memory"); __builtin_amdgcn_sched_barrier(0); } while (0)

DI bf16x8 lds_b128(unsigned addr) { return *(const LAS bf16x8*)(uintptr_t)addr; }
DI u32x4 lds_u128(unsigned addr) { return *(const LAS u32x4*)(uintptr_t)addr; }
DI void lds_w128(unsigned addr, u32x4 v) { *(LAS u32x4*)(uintptr_t)addr = v; }
DI s16x4 lds_tr(unsigned addr) { return __builtin_amdgcn_ds_read_tr16_b64_v4i16((LAS s16x4*)(uintptr_t)addr); }
DI bf16x8 cat(s16x4 a, s16x4 b) { return __builtin_shufflevector(a, b, 0, 1, 2, 3, 4, 5, 6, 7); }
DI void st_sc1(rsrc_t rs, unsigned off, u32x4 v) { __builtin_amdgcn_raw_buffer_store_b128(v, rs, off, 0, 16); }
DI u32x4 ld_sc1(rsrc_t rs, unsigned off) { return __builtin_amdgcn_raw_buffer_load_b128(rs, off, 0, 16); }
DI void st32_dev(void* p, unsigned v) { __hip_atomic_store((GAS unsigned*)p, v, __ATOMIC_RELAXED, __HIP_MEMORY_SCOPE_AGENT); }
DI unsigned ld32_dev(const void* p) { return __hip_atomic_load((GAS unsigned*)p, __ATOMIC_RELAXED, __HIP_MEMORY_SCOPE_AGENT); }
DI void wait_flags(const char* base, unsigned n, unsigned tag, unsigned lane) {
  const unsigned long long t0 = __builtin_amdgcn_s_memrealtime();
  for (;;) {
    const unsigned v = ld32_dev(base + 4 * (lane < n ? lane : 0));
    if (__all(v == tag)) break;
    if (__builtin_amdgcn_s_memrealtime() - t0 > 4000000ull) break;
    __builtin_amdgcn_s_sleep(1);
  }
  asm volatile("" ::: "memory");
}

__global__ __launch_bounds__(512, 2) void k_fused(Params p) {
  extern __shared__ __attribute__((aligned(16))) char smem[];
  const unsigned tid = threadIdx.x, lane = tid & 63, w = rfl(tid >> 6);
  const unsigned wg = blockIdx.x, kvh = wg & 7, j = wg >> 3, br = j >> 4, b = (j >> 1) & 7, sp = j & 1;
  const unsigned grp = br * 8 + b, mem = kvh * 2 + sp;
  const unsigned L0 = (unsigned)(uintptr_t)(LAS char*)smem;
  const unsigned g = w >> 2;
  STAMP(0);
  char* sync = p.ws + WS_SYNC + grp * 1024;
  unsigned tag = 0;
  if (w == 4) tag = ld32_dev(sync) + 1u;

  rsrc_t rx = mk_rsrc(p.x[br], NB * SQ * DM * 2), rwq = mk_rsrc(p.wq[br], DM * HQ * DH * 2), rwo = mk_rsrc(p.wo[br], HQ * DH * DM * 2);
  rsrc_t rk = mk_rsrc(p.k[br], NB * HKV * SK * DH * 2), rv = mk_rsrc(p.v[br], NB * HKV * SK * DH * 2);
  rsrc_t rws = mk_rsrc(p.ws, 64u << 20);

  const unsigned drow = 8 * (w >> 1) + ((lane >> 2) & 7), dch = 8 * (w & 1) + 4 * (lane >> 5) + ((lane & 3) ^ ((drow >> 2) & 3));
  const unsigned voKV = drow * 256 + dch * 16, voWQ = drow * 8192 + dch * 16, voWO = drow * 1024 + dch * 16;
  const unsigned kvbase = (b * HKV + kvh) * SK * 256;
#define TSW 1
  const unsigned tsw = sp * TSW;
  auto kvslot = [&](int u) -> unsigned { return (u & 3) * RSZ; };
  auto issue_k = [&](int u) {
    const unsigned slot = L0 + kvslot(u) + w * 1024, sk = kvbase + (u ^ tsw) * 16384;
#pragma unroll
    for (int i = 0; i < 2; ++i) dma16<KVNT>(rk, voKV, sk + 8192 * i, slot + 8192 * i);
  };
  auto issue_v = [&](int u) {
    const unsigned slot = L0 + kvslot(u) + w * 1024, sv = kvbase + ((u - 1) ^ tsw) * 16384;
#pragma unroll
    for (int i = 0; i < 2; ++i) dma16<KVNT>(rv, voKV, sv + 8192 * i, slot + 16384 + 8192 * i);
  };
  const unsigned wqcol = (kvh * 512 + sp * 256) * 2;
  auto wqslot = [&](int i) -> unsigned { return (i & 3) * RSZ; };
  auto issue_wq = [&](int i) {
    const unsigned kc = (i + b) & 7, slot = L0 + wqslot(i) + w * 1024;
#pragma unroll
    for (int hh = 0; hh < 2; ++hh)
#pragma unroll
      for (int ii = 0; ii < 2; ++ii) dma16<WQNT>(rwq, voWQ, (kc * 64 + 32 * ii) * 8192 + wqcol + hh * 256, slot + hh * 16384 + 8192 * ii);
  };
  const unsigned worow = kvh * 512 + sp * 256;
  auto issue_wo = [&](int i, unsigned slotoff) {
    const unsigned kc = (i + b) & 3, nh = ((i >> 2) ^ (b >> 2)) & 1, slot = L0 + slotoff + w * 1024;
#pragma unroll
    for (int tt = 0; tt < 2; ++tt)
#pragma unroll
      for (int ii = 0; ii < 2; ++ii) dma16<WONT>(rwo, voWO, (worow + kc * 64 + 32 * ii) * 1024 + (2 * nh + tt) * 256, slot + tt * 16384 + 8192 * ii);
  };

  const unsigned h = lane >> 5, r = lane & 31, blk = (lane >> 4) & 1, q4 = (lane & 15) >> 2, pp = lane & 3;
  const unsigned rr0 = 2048 * (r >> 3) + 64 * (r & 7) + 16 * ((0 + h) ^ ((r >> 2) & 3));
  const unsigned rr1 = 2048 * (r >> 3) + 64 * (r & 7) + 16 * ((2 + h) ^ ((r >> 2) & 3));
  const unsigned tp0 = 64 * (4 * h + q4) + 16 * ((2 * blk + (pp >> 1)) ^ (0 + h)) + 8 * (pp & 1);
  const unsigned tp1 = 2048 + 64 * (4 * h + q4) + 16 * ((2 * blk + (pp >> 1)) ^ (2 + h)) + 8 * (pp & 1);
  const unsigned hh = (w >> 1) & 1, kh = w & 1;

  bf16x8 qf[8];
  {
#pragma unroll
    for (int kt = 0; kt < 4; ++kt) dma16<WQNT>(rx, voWO, b * (SQ * DM * 2) + kt * 256, L0 + 4 * RSZ + kt * 8192 + w * 1024);
    issue_wq(0); issue_wq(1); issue_wq(2); issue_wq(3);
    const unsigned ph = w >> 2, c = w & 3;
    const unsigned tn0 = 2048 * h + 64 * q4 + 16 * ((2 * blk + (pp >> 1)) ^ (2 * h)) + 8 * (pp & 1) + 512 * c + ph * 16384;
    const unsigned tn1 = 2048 * h + 256 + 64 * q4 + 16 * ((2 * blk + (pp >> 1)) ^ (2 * h + 1)) + 8 * (pp & 1) + 512 * c + ph * 16384;
    f32x16 acc = {};
#pragma unroll
    for (int i = 0; i < 8; ++i) {
      if (i == 0) WAIT_VM(12); else if (i <= 5) WAIT_VM(8); else if (i == 6) WAIT_VM(6); else WAIT_VM(6);
      BARRIER();
      if (i == 0) STAMP(1);
      if (i >= 1 && i + 3 < 8) issue_wq(i + 3);
      if (i == 5) issue_k(0);
      if (i == 6) { issue_k(1); issue_v(1); }
      if (i == 7) { issue_k(2); issue_v(2); }
      const unsigned kc = (i + b) & 7, slot = L0 + wqslot(i);
      const unsigned xb = L0 + 4 * RSZ + (kc >> 1) * 8192 + (kc & 1) * 1024;
      s16x4 a0[4], a1[4]; bf16x8 xf[4];
#pragma unroll
      for (int ks = 0; ks < 4; ++ks) { a0[ks] = lds_tr(slot + tn0 + 4096 * ks); a1[ks] = lds_tr(slot + tn1 + 4096 * ks); xf[ks] = lds_b128(xb + ((ks & 1) ? rr1 : rr0) + 512 * (ks >> 1)); }
#pragma unroll
      for (int ks = 0; ks < 4; ++ks) acc = MFMA(cat(a0[ks], a1[ks]), xf[ks], acc);
    }
    BARRIER();
    STAMP(2);
    const unsigned QF = L0 + 4 * RSZ;
#pragma unroll
    for (int s = 0; s < 2; ++s) {
      unsigned a0 = cvtpk(acc[8 * s + 0] * SCL, acc[8 * s + 1] * SCL), a1 = cvtpk(acc[8 * s + 2] * SCL, acc[8 * s + 3] * SCL);
      unsigned b0 = cvtpk(acc[8 * s + 4] * SCL, acc[8 * s + 5] * SCL), b1 = cvtpk(acc[8 * s + 6] * SCL, acc[8 * s + 7] * SCL);
      auto r0 = __builtin_amdgcn_permlane32_swap(a0, b0, false, false); auto r1 = __builtin_amdgcn_permlane32_swap(a1, b1, false, false);
      u32x4 o = {r0[0], r1[0], r0[1], r1[1]};
      lds_w128(QF + (ph * 8 + 2 * c + s) * 1024 + lane * 16, o);
    }
    BARRIER();
#pragma unroll
    for (int s = 0; s < 8; ++s) qf[s] = lds_b128(QF + (hh * 8 + s) * 1024 + lane * 16);
    BARRIER();
    issue_wo(0, 4 * RSZ);
  }
  STAMP(3);

  const unsigned kb0 = L0 + 8192 * kh + rr0, kb1 = L0 + 8192 * kh + rr1, vb0 = L0 + 16384 + 8192 * kh + tp0, vb1 = L0 + 16384 + 8192 * kh + tp1;
  f32x16 o[4] = {};
  f32x16 negm = {};
  float m = 0.f, l = 0.f, l2 = 0.f;
  constexpr unsigned DA = 2 * RSZ, DB = 3 * RSZ;
  const unsigned j4 = 2 * g + kh;
  f32x16 own;
  auto deposit = [&]() __attribute__((always_inline)) {
    l = swapadd(l);
    const float inv = 1.0f / l;
#pragma unroll
    for (int c = 0; c < 4; ++c) {
      if (j4 == (unsigned)c) own = o[c];
      else {
        const unsigned d = (j4 - c) & 3;
        const unsigned dst = L0 + (d == 1 ? DB + (hh * 4 + c) * 2048 : DA + ((hh * 4 + c) * 2 + (d - 2)) * 2048) + lane * 16;
#pragma unroll
        for (int s = 0; s < 2; ++s) {
          u32x4 u = {cvtpk(o[c][8 * s] * inv, o[c][8 * s + 1] * inv), cvtpk(o[c][8 * s + 2] * inv, o[c][8 * s + 3] * inv),
                     cvtpk(o[c][8 * s + 4] * inv, o[c][8 * s + 5] * inv), cvtpk(o[c][8 * s + 6] * inv, o[c][8 * s + 7] * inv)};
          lds_w128(dst + s * 1024, u);
        }
      }
    }
    *(LAS float*)(uintptr_t)(L0 + DB + 16384 + (hh * 4 + j4) * 256 + lane * 4) = h ? l : m;
  };
  s16x4 va[4][2] = {}, vc[4][2] = {};
  f32x16 s;
#pragma unroll
  for (int i = 0; i < 16; ++i) s[i] = -__builtin_inff();
  auto finish = [&]() __attribute__((always_inline)) {
#pragma unroll
    for (int ks = 0; ks < 2; ++ks) {
      float e[8];
#pragma unroll
      for (int i = 0; i < 8; ++i) e[i] = __builtin_amdgcn_exp2f(s[8 * ks + i]);
      u32x4 u = {cvtpk(e[0], e[1]), cvtpk(e[2], e[3]), cvtpk(e[4], e[5]), cvtpk(e[6], e[7])};
      const bf16x8 pf = __builtin_bit_cast(bf16x8, u);
#pragma unroll
      for (int c = 0; c < 4; ++c) o[c] = MFMA(cat(va[c][ks], vc[c][ks]), pf, o[c]);
      if (ks == 0) l += ((e[0] + e[1]) + (e[2] + e[3])) + ((e[4] + e[5]) + (e[6] + e[7]));
      else l2 += ((e[0] + e[1]) + (e[2] + e[3])) + ((e[4] + e[5]) + (e[6] + e[7]));
    }
  };
  auto top = [&](int t, auto fast) __attribute__((always_inline)) {
    if constexpr (decltype(fast)::value) WAIT_VM(8);
    else { if (t <= NT - 3) WAIT_VM(8); else if (t < NT) WAIT_VM(6); else WAIT_VM(4); }
    BARRIER();
  };
  auto issue_next = [&](int t, auto fast) __attribute__((always_inline)) {
    if constexpr (decltype(fast)::value) {
      issue_k(t + 3); issue_v(t + 3);
    } else {
      if (t < NT - 3) { issue_k(t + 3); issue_v(t + 3); }
      else if (t == NT - 3) issue_v(NT);
      else if (t == NT - 2) issue_wo(1, 1 * RSZ);
    }
  };
  auto issue_piece = [&](int t, int q, auto fast) __attribute__((always_inline)) {
    if constexpr (decltype(fast)::value) {
      const int u = t + 3;
      const unsigned slot = L0 + kvslot(u) + w * 1024;
      if (q < 2) dma16<KVNT>(rk, voKV, kvbase + (u ^ tsw) * 16384 + 8192 * q, slot + 8192 * q);
      else dma16<KVNT>(rv, voKV, kvbase + ((u - 1) ^ tsw) * 16384 + 8192 * (q - 2), slot + 16384 + 8192 * (q - 2));
    } else { if (q == 0) issue_next(t, fast); }
  };
  auto body = [&](int t, auto fast) __attribute__((always_inline)) {
    top(t, fast);
    __builtin_amdgcn_s_setprio(0);
    if (t == 1) STAMP(5);
    bf16x8 kf[8];
    {
      finish();
      __builtin_amdgcn_sched_barrier(0);
      const unsigned so = kvslot(t);
      const unsigned k0 = kb0 + so, k1 = kb1 + so;
#pragma unroll
      for (int k = 0; k < 8; ++k) kf[k] = lds_b128(((k & 1) ? k1 : k0) + 512 * (k >> 1));
      __builtin_amdgcn_sched_barrier(0);
      issue_next(t, fast);
      __builtin_amdgcn_sched_barrier(0);
    }
    const int t1 = t + 1;
    top(t1, fast);
    __builtin_amdgcn_s_setprio(2);
    if (t1 == 4) { STAMP(4); STAMPC(20); }
    if (t1 == 16) { STAMP(6); STAMPC(21); }
    {
      const unsigned so = kvslot(t1);
      const unsigned v0 = vb0 + so, v1 = vb1 + so;
#pragma unroll
      for (int k = 0; k < 8; ++k) {
        s = MFMA(kf[k], qf[k], k == 0 ? negm : s);
        const int ks = k >> 2, c = k & 3;
        va[c][ks] = lds_tr(v0 + 4096 * ks + 512 * c); vc[c][ks] = lds_tr(v1 + 4096 * ks + 512 * c);
        if (k & 1) issue_piece(t1, k >> 1, fast);
        __builtin_amdgcn_sched_barrier(0);
      }
      const float x0 = fmaxf(fmaxf(s[0], s[1]), s[2]), x1 = fmaxf(fmaxf(s[3], s[4]), s[5]), x2 = fmaxf(fmaxf(s[6], s[7]), s[8]), x3 = fmaxf(fmaxf(s[9], s[10]), s[11]), x4 = fmaxf(fmaxf(s[12], s[13]), s[14]);
      float mx = fmaxf(fmaxf(fmaxf(x0, x1), x2), fmaxf(fmaxf(x3, x4), s[15]));
      mx = swapmax(mx);
      if (__builtin_expect(t < 2 || __any(mx > THR), 0)) {
        const float dl = (t < 2) ? mx : fmaxf(mx, 0.f), al = (t < 2) ? 0.f : __builtin_amdgcn_exp2f(-dl);
        m += dl; l *= al; l2 *= al;
#pragma unroll
        for (int i = 0; i < 16; ++i) s[i] -= dl;
        asm volatile("s_nop 1\n\tv_mfma_f32_32x32x2_f32 %0, %1, %2, 0\n\ts_nop 7\n\ts_nop 7\n\ts_nop 3" : "+v"(negm) : "v"(h ? 0.f : 1.f), "v"(-m));
#pragma unroll
        for (int c = 0; c < 4; ++c) o[c] *= al;
      }
    }
  };
  using FAST = std::integral_constant<bool, true>; using SLOW = std::integral_constant<bool, false>;
  if (g == 1) { top(0, FAST{}); issue_next(0, FAST{}); }
  int t = g;
  for (; t < 28; t += 2) body(t, FAST{});
  for (; t < NT; t += 2) body(t, SLOW{});
  if (g == 0) top(NT, SLOW{});
  __builtin_amdgcn_s_setprio(0);
  finish();
  l += l2;
  STAMP(7);
  deposit();
  BARRIER();
  issue_wo(2, 0 * RSZ);
  {
    u32x4 da[2], db[2], dc[2];
#pragma unroll
    for (int s = 0; s < 2; ++s) {
      da[s] = lds_u128(L0 + DB + (hh * 4 + j4) * 2048 + s * 1024 + lane * 16);
      db[s] = lds_u128(L0 + DA + ((hh * 4 + j4) * 2 + 0) * 2048 + s * 1024 + lane * 16);
      dc[s] = lds_u128(L0 + DA + ((hh * 4 + j4) * 2 + 1) * 2048 + s * 1024 + lane * 16);
    }
    const unsigned mlb = L0 + DB + 16384 + hh * 1024 + lane * 4;
    const float e1 = *(const LAS float*)(uintptr_t)(mlb + ((j4 + 1) & 3) * 256), e2 = *(const LAS float*)(uintptr_t)(mlb + ((j4 + 2) & 3) * 256), e3 = *(const LAS float*)(uintptr_t)(mlb + ((j4 + 3) & 3) * 256);
    const float m1 = lo_half(e1), l1 = hi_half(e1), m2 = lo_half(e2), l2 = hi_half(e2), m3 = lo_half(e3), l3 = hi_half(e3);
    const float M = fmaxf(fmaxf(m, m1), fmaxf(m2, m3));
    float a0 = __builtin_amdgcn_exp2f(m - M), w1 = l1 * __builtin_amdgcn_exp2f(m1 - M), w2 = l2 * __builtin_amdgcn_exp2f(m2 - M), w3 = l3 * __builtin_amdgcn_exp2f(m3 - M);
    const float inv = 1.0f / ((l * a0 + w1) + (w2 + w3));
    a0 *= inv; w1 *= inv; w2 *= inv; w3 *= inv;
#pragma unroll
    for (int s = 0; s < 2; ++s) {
      u32x4 u;
#pragma unroll
      for (int e = 0; e < 4; ++e) {
        const float lo = own[8 * s + 2 * e] * a0 + bflo(da[s][e]) * w1 + bflo(db[s][e]) * w2 + bflo(dc[s][e]) * w3;
        const float hi = own[8 * s + 2 * e + 1] * a0 + bfhi(da[s][e]) * w1 + bfhi(db[s][e]) * w2 + bfhi(dc[s][e]) * w3;
        u[e] = cvtpk(lo, hi);
      }
      lds_w128(L0 + DB + (hh * 4 + j4) * 2048 + s * 1024 + lane * 16, u);
    }
  }
  BARRIER();
  STAMP(10);
  issue_wo(3, DA);
  f32x16 acc[2] = {};
  auto ystore = [&](int half) __attribute__((always_inline)) {
    const unsigned nh = (half ^ (b >> 2)) & 1;
#pragma unroll
    for (int q = 0; q < 2; ++q) {
      u32x4 v = {cvtpk(acc[half][8 * q], acc[half][8 * q + 1]), cvtpk(acc[half][8 * q + 2], acc[half][8 * q + 3]), cvtpk(acc[half][8 * q + 4], acc[half][8 * q + 5]), cvtpk(acc[half][8 * q + 6], acc[half][8 * q + 7])};
      st_sc1(rws, WS_YP + (grp * 16 + mem) * 32768 + ((nh * 8 + w) * 2 + q) * 1024 + lane * 16, v);
    }
  };
  constexpr int YS = 2;
  {
    const unsigned wt0 = (w >> 2) * 16384 + 512 * (w & 3) + tp0, wt1 = (w >> 2) * 16384 + 512 * (w & 3) + tp1;
#pragma unroll
    for (int i = 0; i < 8; ++i) {
      if (i < 5) WAIT_VM(8); else if (i == 5) WAIT_VM(8 + YS); else if (i == 6) WAIT_VM(4 + YS); else WAIT_VM(YS);
      if (i > 0) BARRIER();
      const unsigned so = ((i & 3) == 0 ? 4 : (i & 3) == 1 ? 1 : (i & 3) == 2 ? 0 : 2) * RSZ;
      if (i >= 1 && i + 3 < 8) issue_wo(i + 3, (((i + 3) & 3) == 0 ? 4 : ((i + 3) & 3) == 1 ? 1 : ((i + 3) & 3) == 2 ? 0 : 2) * RSZ);
      if (i == 4) ystore(0);
      const unsigned kc = (i + b) & 3;
      const unsigned ofb = L0 + DB + kc * 4096 + lane * 16;
      s16x4 a0[4], a1[4]; bf16x8 of[4];
#pragma unroll
      for (int q = 0; q < 4; ++q) { a0[q] = lds_tr(L0 + so + wt0 + 4096 * q); a1[q] = lds_tr(L0 + so + wt1 + 4096 * q); of[q] = lds_b128(ofb + q * 1024); }
#pragma unroll
      for (int q = 0; q < 4; ++q) acc[i >> 2] = MFMA(cat(a0[q], a1[q]), of[q], acc[i >> 2]);
    }
  }
  STAMP(11);
  ystore(1);
  WAIT_VM(0);
  BARRIER();
  STAMP(12);
  if (w == 4) {
    if (lane == 0) st32_dev(sync + 128 + 4 * mem, tag);
    wait_flags(sync + 128, 16, tag, lane);
    if (lane == 0 && mem == 0) st32_dev(sync, tag);
    STAMP(13);
  }
  BARRIER();
  if (w < 2) {
    u32x4 v[16];
#pragma unroll
    for (int i = 0; i < 16; ++i) v[i] = ld_sc1(rws, WS_YP + (grp * 16 + i) * 32768 + (mem * 2 + w) * 1024 + lane * 16);
    float a[8] = {};
#pragma unroll
    for (int i = 0; i < 16; ++i)
#pragma unroll
      for (int e = 0; e < 4; ++e) { a[2 * e] += bflo(v[i][e]); a[2 * e + 1] += bfhi(v[i][e]); }
    const unsigned n = 32 * mem + 16 * w + 4 * h;
    u32x2 o0 = {cvtpk(a[0], a[1]), cvtpk(a[2], a[3])}, o1 = {cvtpk(a[4], a[5]), cvtpk(a[6], a[7])};
    char* dst = p.out + ((size_t)(b * 32 + r) * 1024 + br * 512 + n) * 2;
    *(u32x2*)dst = o0; *(u32x2*)(dst + 16) = o1;
  }
  STAMP(14);
}

extern "C" void kernel_launch(void* const* d_in, const int* in_sizes, int n_in, void* d_out, int out_size, void* d_ws, size_t ws_size, hipStream_t stream) {
  Params p;
  for (int i = 0; i < 2; ++i) {
    p.x[i] = (const char*)d_in[5 * i + 0]; p.k[i] = (const char*)d_in[5 * i + 1]; p.v[i] = (const char*)d_in[5 * i + 2];
    p.wq[i] = (const char*)d_in[5 * i + 3]; p.wo[i] = (const char*)d_in[5 * i + 4];
  }
  p.out = (char*)d_out; p.ws = (char*)d_ws;
  constexpr int SMEM = 5 * 32768;
  (void)hipFuncSetAttribute((const void*)k_fused, hipFuncAttributeMaxDynamicSharedMemorySize, SMEM);
  hipLaunchKernelGGL(k_fused, dim3(256), dim3(512), SMEM, stream, p);
}
```

```cpp
#include <hip/hip_runtime.h>
#include <stdint.h>
#include <type_traits>

typedef __attribute__((ext_vector_type(8))) short bf16x8;
typedef __attribute__((ext_vector_type(4))) short s16x4;
typedef __attribute__((ext_vector_type(16))) float f32x16;
typedef __attribute__((ext_vector_type(4))) float f32x4;
typedef __attribute__((ext_vector_type(2))) float f32x2;
typedef __attribute__((ext_vector_type(4))) unsigned u32x4;
typedef __attribute__((ext_vector_type(2))) unsigned u32x2;
typedef __attribute__((ext_vector_type(2))) __bf16 bf16x2;
#define LAS __attribute__((address_space(3)))
#define GAS __attribute__((address_space(1)))
#define DI __device__ __forceinline__
#define MFMA(a, b, c) __builtin_amdgcn_mfma_f32_32x32x16_bf16((a), (b), (c), 0, 0, 0)

constexpr int NB = 8, SQ = 32, DM = 512, HQ = 32, HKV = 8, SK = 2048, DH = 128;
constexpr float SCL = 0.088388347648318447f * 1.4426950408889634f;
constexpr float THR = 8.0f;
constexpr int NT = 32;
constexpr unsigned RSZ = 32768;

struct Params {
  const char* x[2]; const char* k[2]; const char* v[2]; const char* wq[2]; const char* wo[2];
  char* out; char* ws;
};
constexpr size_t WS_SYNC = 0;
constexpr size_t WS_YP = 1u << 20;

DI unsigned rfl(unsigned v) { return __builtin_amdgcn_readfirstlane(v); }
DI unsigned cvtpk(float lo, float hi) { f32x2 v = {lo, hi}; bf16x2 b = __builtin_convertvector(v, bf16x2); return __builtin_bit_cast(unsigned, b); }
DI float bflo(unsigned u) { return __uint_as_float(u << 16); }
DI float bfhi(unsigned u) { return __uint_as_float(u & 0xffff0000u); }
DI float swapmax(float x) { auto r = __builtin_amdgcn_permlane32_swap(__float_as_uint(x), __float_as_uint(x), false, false); return fmaxf(__uint_as_float(r[0]), __uint_as_float(r[1])); }
DI float swapadd(float x) { auto r = __builtin_amdgcn_permlane32_swap(__float_as_uint(x), __float_as_uint(x), false, false); return __uint_as_float(r[0]) + __uint_as_float(r[1]); }
DI float lo_half(float x) { auto r = __builtin_amdgcn_permlane32_swap(__float_as_uint(x), __float_as_uint(x), false, false); return __uint_as_float(r[0]); }
DI float hi_half(float x) { auto r = __builtin_amdgcn_permlane32_swap(__float_as_uint(x), __float_as_uint(x), false, false); return __uint_as_float(r[1]); }

typedef __amdgpu_buffer_rsrc_t rsrc_t;
DI rsrc_t mk_rsrc(const void* p, unsigned bytes) { return __builtin_amdgcn_make_buffer_rsrc((void*)p, 0, bytes, 0x00020000); }
DI void dma16(rsrc_t rs, unsigned voff, unsigned soff, unsigned ldsdst) {
  asm volatile("s_mov_b32 m0, %0\n\ts_nop 0\n\tbuffer_load_dwordx4 %1, %2, %3 offen lds" :: "s"(ldsdst), "v"(voff), "s"(rs), "s"(soff) : "memory");
}
#define WAIT_VM(n) asm volatile("s_waitcnt vmcnt(%0)" :: "i"(n) : "memory")
#define BARRIER() do { __builtin_amdgcn_sched_barrier(0); asm volatile("" ::: "memory"); __builtin_amdgcn_s_waitcnt(0xC07F); __builtin_amdgcn_s_barrier(); asm volatile("" ::: "memory"); __builtin_amdgcn_sched_barrier(0); } while (0)

DI bf16x8 lds_b128(unsigned addr) { return *(const LAS bf16x8*)(uintptr_t)addr; }
DI u32x4 lds_u128(unsigned addr) { return *(const LAS u32x4*)(uintptr_t)addr; }
DI void lds_w128(unsigned addr, u32x4 v) { *(LAS u32x4*)(uintptr_t)addr = v; }
DI s16x4 lds_tr(unsigned addr) { return __builtin_amdgcn_ds_read_tr16_b64_v4i16((LAS s16x4*)(uintptr_t)addr); }
DI bf16x8 cat(s16x4 a, s16x4 b) { return __builtin_shufflevector(a, b, 0, 1, 2, 3, 4, 5, 6, 7); }
DI void st_sc1(rsrc_t rs, unsigned off, u32x4 v) { __builtin_amdgcn_raw_buffer_store_b128(v, rs, off, 0, 16); }
DI u32x4 ld_sc1(rsrc_t rs, unsigned off) { return __builtin_amdgcn_raw_buffer_load_b128(rs, off, 0, 16); }
DI void st32_dev(void* p, unsigned v) { __hip_atomic_store((GAS unsigned*)p, v, __ATOMIC_RELAXED, __HIP_MEMORY_SCOPE_AGENT); }
DI unsigned ld32_dev(const void* p) { return __hip_atomic_load((GAS unsigned*)p, __ATOMIC_RELAXED, __HIP_MEMORY_SCOPE_AGENT); }
DI void wait_flags(const char* base, unsigned n, unsigned tag, unsigned lane) {
  const unsigned long long t0 = __builtin_amdgcn_s_memrealtime();
  for (;;) {
    const unsigned v = ld32_dev(base + 4 * (lane < n ? lane : 0));
    if (__all(v == tag)) break;
    if (__builtin_amdgcn_s_memrealtime() - t0 > 4000000ull) break;
    __builtin_amdgcn_s_sleep(1);
  }
  asm volatile("" ::: "memory");
}

__global__ __launch_bounds__(512, 2) void k_fused(Params p) {
  extern __shared__ __attribute__((aligned(16))) char smem[];
  const unsigned tid = threadIdx.x, lane = tid & 63, w = rfl(tid >> 6);
  const unsigned wg = blockIdx.x, kvh = wg & 7, j = wg >> 3, br = j >> 4, b = (j >> 1) & 7, sp = j & 1;
  const unsigned grp = br * 8 + b, mem = kvh * 2 + sp;
  const unsigned L0 = (unsigned)(uintptr_t)(LAS char*)smem;
  const unsigned g = w >> 2;
  char* sync = p.ws + WS_SYNC + grp * 1024;

  unsigned tag;
  rsrc_t rx = mk_rsrc(p.x[br], NB * SQ * DM * 2), rwq = mk_rsrc(p.wq[br], DM * HQ * DH * 2), rwo = mk_rsrc(p.wo[br], HQ * DH * DM * 2);
  rsrc_t rk = mk_rsrc(p.k[br], NB * HKV * SK * DH * 2), rv = mk_rsrc(p.v[br], NB * HKV * SK * DH * 2);
  rsrc_t rws = mk_rsrc(p.ws, 16u << 20);

  const unsigned drow = 8 * (w >> 1) + ((lane >> 2) & 7), dch = 8 * (w & 1) + 4 * (lane >> 5) + ((lane & 3) ^ ((drow >> 2) & 3));
  const unsigned voKV = drow * 256 + dch * 16, voWQ = drow * 8192 + dch * 16, voWO = drow * 1024 + dch * 16;
  const unsigned kvbase = (b * HKV + kvh) * SK * 256;
  const unsigned tsw = sp;
  auto kvslot = [&](int u) -> unsigned { return (u & 3) * RSZ; };
  auto issue_k = [&](int u) {
    const unsigned slot = L0 + kvslot(u) + w * 1024, sk = kvbase + (u ^ tsw) * 16384;
#pragma unroll
    for (int i = 0; i < 2; ++i) dma16(rk, voKV, sk + 8192 * i, slot + 8192 * i);
  };
  auto issue_v = [&](int u) {
    const unsigned slot = L0 + kvslot(u) + w * 1024, sv = kvbase + ((u - 1) ^ tsw) * 16384;
#pragma unroll
    for (int i = 0; i < 2; ++i) dma16(rv, voKV, sv + 8192 * i, slot + 16384 + 8192 * i);
  };
  const unsigned wqcol = (kvh * 512 + sp * 256) * 2;
  auto wqslot = [&](int i) -> unsigned { return (i & 3) * RSZ; };
  auto issue_wq = [&](int i) {
    const unsigned kc = (i + b) & 7, slot = L0 + wqslot(i) + w * 1024;
#pragma unroll
    for (int hh = 0; hh < 2; ++hh)
#pragma unroll
      for (int ii = 0; ii < 2; ++ii) dma16(rwq, voWQ, (kc * 64 + 32 * ii) * 8192 + wqcol + hh * 256, slot + hh * 16384 + 8192 * ii);
  };
  const unsigned worow = kvh * 512 + sp * 256;
  auto issue_wo = [&](int i, unsigned slotoff) {
    const unsigned kc = (i + b) & 3, nh = ((i >> 2) ^ (b >> 2)) & 1, slot = L0 + slotoff + w * 1024;
#pragma unroll
    for (int tt = 0; tt < 2; ++tt)
#pragma unroll
      for (int ii = 0; ii < 2; ++ii) dma16(rwo, voWO, (worow + kc * 64 + 32 * ii) * 1024 + (2 * nh + tt) * 256, slot + tt * 16384 + 8192 * ii);
  };

  const unsigned h = lane >> 5, r = lane & 31, blk = (lane >> 4) & 1, q4 = (lane & 15) >> 2, pp = lane & 3;
  const unsigned rr0 = 2048 * (r >> 3) + 64 * (r & 7) + 16 * ((0 + h) ^ ((r >> 2) & 3));
  const unsigned rr1 = 2048 * (r >> 3) + 64 * (r & 7) + 16 * ((2 + h) ^ ((r >> 2) & 3));
  const unsigned tp0 = 64 * (4 * h + q4) + 16 * ((2 * blk + (pp >> 1)) ^ (0 + h)) + 8 * (pp & 1);
  const unsigned tp1 = 2048 + 64 * (4 * h + q4) + 16 * ((2 * blk + (pp >> 1)) ^ (2 + h)) + 8 * (pp & 1);
  const unsigned hh = (w >> 1) & 1, kh = w & 1;

  bf16x8 qf[8];
  {
#pragma unroll
    for (int kt = 0; kt < 4; ++kt) dma16(rx, voWO, b * (SQ * DM * 2) + kt * 256, L0 + 4 * RSZ + kt * 8192 + w * 1024);
    issue_wq(0); issue_wq(1); issue_wq(2); issue_wq(3);
    asm volatile("global_load_dword %0, %1, off sc1" : "=v"(tag) : "v"(sync) : "memory");
    const unsigned ph = w >> 2, c = w & 3;
    const unsigned tn0 = 2048 * h + 64 * q4 + 16 * ((2 * blk + (pp >> 1)) ^ (2 * h)) + 8 * (pp & 1) + 512 * c + ph * 16384;
    const unsigned tn1 = 2048 * h + 256 + 64 * q4 + 16 * ((2 * blk + (pp >> 1)) ^ (2 * h + 1)) + 8 * (pp & 1) + 512 * c + ph * 16384;
    f32x16 acc = {};
#pragma unroll
    for (int i = 0; i < 8; ++i) {
      if (i == 0) WAIT_VM(13); else if (i <= 3) WAIT_VM(9); else if (i <= 5) WAIT_VM(8); else WAIT_VM(6);
      if (i == 4) { asm volatile("" : "+v"(tag)); tag += 1u; }
      BARRIER();
      const unsigned kc = (i + b) & 7, slot = L0 + wqslot(i);
      const unsigned xb = L0 + 4 * RSZ + (kc >> 1) * 8192 + (kc & 1) * 1024;
      s16x4 a0[4], a1[4]; bf16x8 xf[4];
#pragma unroll
      for (int ks = 0; ks < 4; ++ks) { a0[ks] = lds_tr(slot + tn0 + 4096 * ks); a1[ks] = lds_tr(slot + tn1 + 4096 * ks); xf[ks] = lds_b128(xb + ((ks & 1) ? rr1 : rr0) + 512 * (ks >> 1)); }
      __builtin_amdgcn_sched_barrier(0);
#pragma unroll
      for (int ks = 0; ks < 4; ++ks) acc = MFMA(cat(a0[ks], a1[ks]), xf[ks], acc);
      __builtin_amdgcn_sched_barrier(0);
      if (i >= 1 && i + 3 < 8) issue_wq(i + 3);
      if (i == 5) issue_k(0);
      if (i == 6) { issue_k(1); issue_v(1); }
      if (i == 7) { issue_k(2); issue_v(2); }
    }
    BARRIER();
    const unsigned QF = L0 + 4 * RSZ;
#pragma unroll
    for (int s = 0; s < 2; ++s) {
      unsigned a0 = cvtpk(acc[8 * s + 0] * SCL, acc[8 * s + 1] * SCL), a1 = cvtpk(acc[8 * s + 2] * SCL, acc[8 * s + 3] * SCL);
      unsigned b0 = cvtpk(acc[8 * s + 4] * SCL, acc[8 * s + 5] * SCL), b1 = cvtpk(acc[8 * s + 6] * SCL, acc[8 * s + 7] * SCL);
      auto r0 = __builtin_amdgcn_permlane32_swap(a0, b0, false, false); auto r1 = __builtin_amdgcn_permlane32_swap(a1, b1, false, false);
      u32x4 o = {r0[0], r1[0], r0[1], r1[1]};
      lds_w128(QF + (ph * 8 + 2 * c + s) * 1024 + lane * 16, o);
    }
    BARRIER();
#pragma unroll
    for (int s = 0; s < 8; ++s) qf[s] = lds_b128(QF + (hh * 8 + s) * 1024 + lane * 16);
    BARRIER();
    issue_wo(0, 4 * RSZ);
  }

  const unsigned kb0 = L0 + 8192 * kh + rr0, kb1 = L0 + 8192 * kh + rr1, vb0 = L0 + 16384 + 8192 * kh + tp0, vb1 = L0 + 16384 + 8192 * kh + tp1;
  f32x16 o[4] = {};
  f32x16 negm = {};
  float m = 0.f, l = 0.f, l2 = 0.f;
  constexpr unsigned DA = 2 * RSZ, DB = 3 * RSZ;
  const unsigned j4 = 2 * g + kh;
  f32x16 own;
  auto deposit = [&]() __attribute__((always_inline)) {
    l = swapadd(l);
    const float inv = 1.0f / l;
#pragma unroll
    for (int c = 0; c < 4; ++c) {
      if (j4 == (unsigned)c) own = o[c];
      else {
        const unsigned d = (j4 - c) & 3;
        const unsigned dst = L0 + (d == 1 ? DB + (hh * 4 + c) * 2048 : DA + ((hh * 4 + c) * 2 + (d - 2)) * 2048) + lane * 16;
#pragma unroll
        for (int s = 0; s < 2; ++s) {
          u32x4 u = {cvtpk(o[c][8 * s] * inv, o[c][8 * s + 1] * inv), cvtpk(o[c][8 * s + 2] * inv, o[c][8 * s + 3] * inv),
                     cvtpk(o[c][8 * s + 4] * inv, o[c][8 * s + 5] * inv), cvtpk(o[c][8 * s + 6] * inv, o[c][8 * s + 7] * inv)};
          lds_w128(dst + s * 1024, u);
        }
      }
    }
    *(LAS float*)(uintptr_t)(L0 + DB + 16384 + (hh * 4 + j4) * 256 + lane * 4) = h ? l : m;
  };
  s16x4 va[4][2] = {}, vc[4][2] = {};
  f32x16 s;
#pragma unroll
  for (int i = 0; i < 16; ++i) s[i] = -__builtin_inff();
  auto finish = [&]() __attribute__((always_inline)) {
#pragma unroll
    for (int ks = 0; ks < 2; ++ks) {
      float e[8];
#pragma unroll
      for (int i = 0; i < 8; ++i) e[i] = __builtin_amdgcn_exp2f(s[8 * ks + i]);
      u32x4 u = {cvtpk(e[0], e[1]), cvtpk(e[2], e[3]), cvtpk(e[4], e[5]), cvtpk(e[6], e[7])};
      const bf16x8 pf = __builtin_bit_cast(bf16x8, u);
#pragma unroll
      for (int c = 0; c < 4; ++c) o[c] = MFMA(cat(va[c][ks], vc[c][ks]), pf, o[c]);
      if (ks == 0) l += ((e[0] + e[1]) + (e[2] + e[3])) + ((e[4] + e[5]) + (e[6] + e[7]));
      else l2 += ((e[0] + e[1]) + (e[2] + e[3])) + ((e[4] + e[5]) + (e[6] + e[7]));
    }
  };
  auto top = [&](int t, auto fast) __attribute__((always_inline)) {
    if constexpr (decltype(fast)::value) WAIT_VM(8);
    else { if (t <= NT - 3) WAIT_VM(8); else if (t < NT) WAIT_VM(6); else WAIT_VM(4); }
    BARRIER();
  };
  auto issue_next = [&](int t, auto fast) __attribute__((always_inline)) {
    if constexpr (decltype(fast)::value) {
      issue_k(t + 3); issue_v(t + 3);
    } else {
      if (t < NT - 3) { issue_k(t + 3); issue_v(t + 3); }
      else if (t == NT - 3) issue_v(NT);
      else if (t == NT - 2) issue_wo(1, 1 * RSZ);
    }
  };
  auto issue_piece = [&](int t, int q, auto fast) __attribute__((always_inline)) {
    if constexpr (decltype(fast)::value) {
      const int u = t + 3;
      const unsigned slot = L0 + kvslot(u) + w * 1024;
      if (q < 2) dma16(rk, voKV, kvbase + (u ^ tsw) * 16384 + 8192 * q, slot + 8192 * q);
      else dma16(rv, voKV, kvbase + ((u - 1) ^ tsw) * 16384 + 8192 * (q - 2), slot + 16384 + 8192 * (q - 2));
    } else { if (q == 0) issue_next(t, fast); }
  };
  auto body = [&](int t, auto fast) __attribute__((always_inline)) {
    top(t, fast);
    __builtin_amdgcn_s_setprio(0);
    bf16x8 kf[8];
    {
      finish();
      __builtin_amdgcn_sched_barrier(0);
      const unsigned so = kvslot(t);
      const unsigned k0 = kb0 + so, k1 = kb1 + so;
#pragma unroll
      for (int k = 0; k < 8; ++k) kf[k] = lds_b128(((k & 1) ? k1 : k0) + 512 * (k >> 1));
      __builtin_amdgcn_sched_barrier(0);
      issue_next(t, fast);
      __builtin_amdgcn_sched_barrier(0);
    }
    const int t1 = t + 1;
    top(t1, fast);
    __builtin_amdgcn_s_setprio(2);
    {
      const unsigned so = kvslot(t1);
      const unsigned v0 = vb0 + so, v1 = vb1 + so;
#pragma unroll
      for (int k = 0; k < 8; ++k) {
        s = MFMA(kf[k], qf[k], k == 0 ? negm : s);
        const int ks = k >> 2, c = k & 3;
        va[c][ks] = lds_tr(v0 + 4096 * ks + 512 * c); vc[c][ks] = lds_tr(v1 + 4096 * ks + 512 * c);
        if (k & 1) issue_piece(t1, k >> 1, fast);
        __builtin_amdgcn_sched_barrier(0);
      }
      const float x0 = fmaxf(fmaxf(s[0], s[1]), s[2]), x1 = fmaxf(fmaxf(s[3], s[4]), s[5]), x2 = fmaxf(fmaxf(s[6], s[7]), s[8]), x3 = fmaxf(fmaxf(s[9], s[10]), s[11]), x4 = fmaxf(fmaxf(s[12], s[13]), s[14]);
      float mx = fmaxf(fmaxf(fmaxf(x0, x1), x2), fmaxf(fmaxf(x3, x4), s[15]));
      mx = swapmax(mx);
      if (__builtin_expect(t < 2 || __any(mx > THR), 0)) {
        const float dl = (t < 2) ? mx : fmaxf(mx, 0.f), al = (t < 2) ? 0.f : __builtin_amdgcn_exp2f(-dl);
        m += dl; l *= al; l2 *= al;
#pragma unroll
        for (int i = 0; i < 16; ++i) s[i] -= dl;
        asm volatile("s_nop 1\n\tv_mfma_f32_32x32x2_f32 %0, %1, %2, 0\n\ts_nop 7\n\ts_nop 7\n\ts_nop 3" : "+v"(negm) : "v"(h ? 0.f : 1.f), "v"(-m));
#pragma unroll
        for (int c = 0; c < 4; ++c) o[c] *= al;
      }
    }
  };
  using FAST = std::integral_constant<bool, true>; using SLOW = std::integral_constant<bool, false>;
  if (g == 1) { top(0, FAST{}); issue_next(0, FAST{}); }
  int t = g;
  for (; t < 28; t += 2) body(t, FAST{});
  for (; t < NT; t += 2) body(t, SLOW{});
  if (g == 0) top(NT, SLOW{});
  __builtin_amdgcn_s_setprio(0);
  finish();
  l += l2;
  deposit();
  BARRIER();
  issue_wo(2, 0 * RSZ);
  {
    u32x4 da[2], db[2], dc[2];
#pragma unroll
    for (int s = 0; s < 2; ++s) {
      da[s] = lds_u128(L0 + DB + (hh * 4 + j4) * 2048 + s * 1024 + lane * 16);
      db[s] = lds_u128(L0 + DA + ((hh * 4 + j4) * 2 + 0) * 2048 + s * 1024 + lane * 16);
      dc[s] = lds_u128(L0 + DA + ((hh * 4 + j4) * 2 + 1) * 2048 + s * 1024 + lane * 16);
    }
    const unsigned mlb = L0 + DB + 16384 + hh * 1024 + lane * 4;
    const float e1 = *(const LAS float*)(uintptr_t)(mlb + ((j4 + 1) & 3) * 256), e2 = *(const LAS float*)(uintptr_t)(mlb + ((j4 + 2) & 3) * 256), e3 = *(const LAS float*)(uintptr_t)(mlb + ((j4 + 3) & 3) * 256);
    const float m1 = lo_half(e1), l1 = hi_half(e1), m2 = lo_half(e2), l2 = hi_half(e2), m3 = lo_half(e3), l3 = hi_half(e3);
    const float M = fmaxf(fmaxf(m, m1), fmaxf(m2, m3));
    float a0 = __builtin_amdgcn_exp2f(m - M), w1 = l1 * __builtin_amdgcn_exp2f(m1 - M), w2 = l2 * __builtin_amdgcn_exp2f(m2 - M), w3 = l3 * __builtin_amdgcn_exp2f(m3 - M);
    const float inv = 1.0f / ((l * a0 + w1) + (w2 + w3));
    a0 *= inv; w1 *= inv; w2 *= inv; w3 *= inv;
#pragma unroll
    for (int s = 0; s < 2; ++s) {
      u32x4 u;
#pragma unroll
      for (int e = 0; e < 4; ++e) {
        const float lo = own[8 * s + 2 * e] * a0 + bflo(da[s][e]) * w1 + bflo(db[s][e]) * w2 + bflo(dc[s][e]) * w3;
        const float hi = own[8 * s + 2 * e + 1] * a0 + bfhi(da[s][e]) * w1 + bfhi(db[s][e]) * w2 + bfhi(dc[s][e]) * w3;
        u[e] = cvtpk(lo, hi);
      }
      lds_w128(L0 + DB + (hh * 4 + j4) * 2048 + s * 1024 + lane * 16, u);
    }
  }
  BARRIER();
  issue_wo(3, DA);
  f32x16 acc[2] = {};
  auto ystore = [&](int half) __attribute__((always_inline)) {
    const unsigned nh = (half ^ (b >> 2)) & 1;
#pragma unroll
    for (int q = 0; q < 2; ++q) {
      u32x4 v = {cvtpk(acc[half][8 * q], acc[half][8 * q + 1]), cvtpk(acc[half][8 * q + 2], acc[half][8 * q + 3]), cvtpk(acc[half][8 * q + 4], acc[half][8 * q + 5]), cvtpk(acc[half][8 * q + 6], acc[half][8 * q + 7])};
      st_sc1(rws, WS_YP + (grp * 16 + mem) * 32768 + ((nh * 8 + w) * 2 + q) * 1024 + lane * 16, v);
    }
  };
  constexpr int YS = 2;
  {
    const unsigned wt0 = (w >> 2) * 16384 + 512 * (w & 3) + tp0, wt1 = (w >> 2) * 16384 + 512 * (w & 3) + tp1;
#pragma unroll
    for (int i = 0; i < 8; ++i) {
      if (i < 5) WAIT_VM(8); else if (i == 5) WAIT_VM(8 + YS); else if (i == 6) WAIT_VM(4 + YS); else WAIT_VM(YS);
      if (i > 0) BARRIER();
      const unsigned so = ((i & 3) == 0 ? 4 : (i & 3) == 1 ? 1 : (i & 3) == 2 ? 0 : 2) * RSZ;
      const unsigned kc = (i + b) & 3;
      const unsigned ofb = L0 + DB + kc * 4096 + lane * 16;
      s16x4 a0[4], a1[4]; bf16x8 of[4];
#pragma unroll
      for (int q = 0; q < 4; ++q) { a0[q] = lds_tr(L0 + so + wt0 + 4096 * q); a1[q] = lds_tr(L0 + so + wt1 + 4096 * q); of[q] = lds_b128(ofb + q * 1024); }
      __builtin_amdgcn_sched_barrier(0);
#pragma unroll
      for (int q = 0; q < 4; ++q) acc[i >> 2] = MFMA(cat(a0[q], a1[q]), of[q], acc[i >> 2]);
      __builtin_amdgcn_sched_barrier(0);
      if (i >= 1 && i + 3 < 8) issue_wo(i + 3, (((i + 3) & 3) == 0 ? 4 : ((i + 3) & 3) == 1 ? 1 : ((i + 3) & 3) == 2 ? 0 : 2) * RSZ);
      if (i == 4) ystore(0);
    }
  }
  ystore(1);
  WAIT_VM(0);
  BARRIER();
  if (w == 4) {
    if (lane == 0) st32_dev(sync + 128 + 4 * mem, tag);
    wait_flags(sync + 128, 16, tag, lane);
    if (lane == 0 && mem == 0) st32_dev(sync, tag);
  }
  BARRIER();
  if (w < 2) {
    u32x4 v[16];
#pragma unroll
    for (int i = 0; i < 16; ++i) v[i] = ld_sc1(rws, WS_YP + (grp * 16 + i) * 32768 + (mem * 2 + w) * 1024 + lane * 16);
    float a[8] = {};
#pragma unroll
    for (int i = 0; i < 16; ++i)
#pragma unroll
      for (int e = 0; e < 4; ++e) { a[2 * e] += bflo(v[i][e]); a[2 * e + 1] += bfhi(v[i][e]); }
    const unsigned n = 32 * mem + 16 * w + 4 * h;
    u32x2 o0 = {cvtpk(a[0], a[1]), cvtpk(a[2], a[3])}, o1 = {cvtpk(a[4], a[5]), cvtpk(a[6], a[7])};
    char* dst = p.out + ((size_t)(b * 32 + r) * 1024 + br * 512 + n) * 2;
    *(u32x2*)dst = o0; *(u32x2*)(dst + 16) = o1;
  }
}

extern "C" void kernel_launch(void* const* d_in, const int* in_sizes, int n_in, void* d_out, int out_size, void* d_ws, size_t ws_size, hipStream_t stream) {
  Params p;
  for (int i = 0; i < 2; ++i) {
    p.x[i] = (const char*)d_in[5 * i + 0]; p.k[i] = (const char*)d_in[5 * i + 1]; p.v[i] = (const char*)d_in[5 * i + 2];
    p.wq[i] = (const char*)d_in[5 * i + 3]; p.wo[i] = (const char*)d_in[5 * i + 4];
  }
  p.out = (char*)d_out; p.ws = (char*)d_ws;
  constexpr int SMEM = 5 * 32768;
  (void)hipFuncSetAttribute((const void*)k_fused, hipFuncAttributeMaxDynamicSharedMemorySize, SMEM);
  hipLaunchKernelGGL(k_fused, dim3(256), dim3(512), SMEM, stream, p);
}
```
